```python
import torch

def _softmax(x, axis=-1):
    return torch.softmax(x, dim=axis)

def _gelu(x):
    return torch.nn.functional.gelu(x, approximate='tanh')

def _silu(x):
    return torch.nn.functional.silu(x)

def _sigmoid(x):
    return torch.sigmoid(x)

def _layernorm(x, axis=-1, eps=1e-05):
    m = x.mean(dim=axis, keepdim=True)
    v = x.var(dim=axis, keepdim=True, unbiased=False)
    return (x - m) / torch.sqrt(v + eps)

def _scatter_add(tgt, idx, src, axis=0):
    return tgt.clone().index_add_(axis, idx, src)

def _segment_sum(src, seg, n):
    out = torch.zeros((n,) + src.shape[1:], dtype=src.dtype)
    return out.index_add_(0, seg, src)

def _pad(x, pw):
    flat = tuple((v for pair in reversed(pw) for v in pair))
    return torch.nn.functional.pad(x, flat)
kernel_name = 'parallel_sum__memory_aug_recurrent_mem_tr__moe_arch_mod__prompt'

def setup_inputs():

    _w = lambda a: (lambda t: t.to(torch.bfloat16) if t.is_floating_point() else t)(torch.as_tensor(a))

    def _A_setup():
        B, S, Dm = (1, 2048, 640)
        H, Dh = (32, 128)
        M, seg = (8, 32)
        seg = min(seg, S)
        S = S // seg * seg
        return {'x': _w((lambda *s: torch.randn(*s, dtype=torch.bfloat16) * 0.05)(B, S, Dm)), 'mem0': _w((lambda *s: torch.randn(*s, dtype=torch.bfloat16) * 1.6)(B, M, Dm)), 'Wq': _w((lambda *s: torch.randn(*s, dtype=torch.bfloat16) * 0.8)(Dm, H, Dh) * Dm ** (-0.5)), 'Wk': _w((lambda *s: torch.randn(*s, dtype=torch.bfloat16) * 0.8)(Dm, H, Dh) * Dm ** (-0.5)), 'Wv': _w((lambda *s: torch.randn(*s, dtype=torch.bfloat16) * 1.6)(Dm, H, Dh) * Dm ** (-0.5)), 'Wo': _w((lambda *s: torch.randn(*s, dtype=torch.bfloat16) * 1.6)(H, Dh, Dm) * (H * Dh) ** (-0.5)), 'seg_len': seg}
    _A_in = _A_setup()

    def _B_setup():
        B, S, D, F, C = (1, 2048, 640, 1728, 8)
        return {'x': _w((lambda *s: torch.randn(*s, dtype=torch.bfloat16) * 0.05)(B, S, D)), 'w_r': _w((lambda *s: torch.randn(*s, dtype=torch.bfloat16) * 0.05)(D)), 'w_gate': _w((lambda *s: torch.randn(*s, dtype=torch.bfloat16) * 0.05)(D, F)), 'w_up': _w((lambda *s: torch.randn(*s, dtype=torch.bfloat16) * 0.05)(D, F)), 'w_down': _w((lambda *s: torch.randn(*s, dtype=torch.bfloat16) * 0.05)(F, D))}
    _B_in = _B_setup()
    return {'A_x': _A_in['x'], 'A_mem0': _A_in['mem0'], 'A_Wq': _A_in['Wq'], 'A_Wk': _A_in['Wk'], 'A_Wv': _A_in['Wv'], 'A_Wo': _A_in['Wo'], 'A_seg_len': _A_in['seg_len'], 'B_x': _B_in['x'], 'B_w_r': _B_in['w_r'], 'B_w_gate': _B_in['w_gate'], 'B_w_up': _B_in['w_up'], 'B_w_down': _B_in['w_down']}

def reference(A_x, A_mem0, A_Wq, A_Wk, A_Wv, A_Wo, A_seg_len, B_x, B_w_r, B_w_gate, B_w_up, B_w_down):

    def _A_ref(x, mem0, Wq, Wk, Wv, Wo, seg_len):
        B, S, Dm = x.shape
        H, Dh = (Wq.shape[1], Wq.shape[2])
        M = mem0.shape[1]
        scale = Dh ** (-0.5)
        n_seg = S // seg_len
        L = M + seg_len
        idx = torch.arange(L)
        mask = (idx[:, None] < M) | (idx[:, None] >= idx[None, :])
        mem = mem0
        outs = []
        for s in range(n_seg):
            xs = x[:, s * seg_len:(s + 1) * seg_len, :].clone()
            h = torch.concatenate([mem, xs], axis=1)
            q = torch.einsum('btd,dhe->bhte', h, Wq)
            k = torch.einsum('btd,dhe->bhte', h, Wk)
            v = torch.einsum('btd,dhe->bhte', h, Wv)
            scores = torch.einsum('bhqe,bhke->bhqk', q, k) * scale
            scores = torch.where(mask[None, None], scores, -1000000000.0)
            p = _softmax(scores, axis=-1)
            o = torch.einsum('bhqk,bhke->bhqe', p, v)
            o = torch.einsum('bhte,hed->btd', o, Wo)
            mem = o[:, :M, :].clone()
            outs.append(o[:, M:, :])
        return torch.concatenate(outs, axis=1)

    def _B_ref(x, w_r, w_gate, w_up, w_down):
        B, S, D = x.shape
        C = 8
        r = x @ w_r
        idx = torch.argsort(-r, axis=-1)[:, :C]
        idx_e = (lambda x, n, axis: torch.repeat_interleave(x, n, dim=axis))(idx.reshape(B, C, 1), D, axis=2)
        x_sel = (lambda x, i, axis: torch.take_along_dim(x, i, dim=axis))(x, idx_e, axis=1)
        g = _silu(x_sel @ w_gate)
        u = x_sel @ w_up
        y = g * u @ w_down
        r_sel = _sigmoid((lambda x, i, axis: torch.take_along_dim(x, i, dim=axis))(r, idx, axis=1))
        y = y * r_sel.reshape(B, C, 1)
        b_off = (torch.arange(B) * S).reshape(B, 1)
        flat_idx = (idx + b_off).reshape(B * C)
        zero = (x * 0.0).reshape(B * S, D)
        upd = _scatter_add(zero, flat_idx, y.reshape(B * C, D), axis=0)
        return x + upd.reshape(B, S, D)
    x = A_x
    xn = _layernorm(x, axis=-1)
    return x + _A_ref(xn, A_mem0, A_Wq, A_Wk, A_Wv, A_Wo, A_seg_len) + _B_ref(xn, B_w_r, B_w_gate, B_w_up, B_w_down)
```

```cpp
#include <hip/hip_runtime.h>
#include <hip/hip_bf16.h>
#include <stdint.h>

#define S_TOK 2048
#define DM 640
#define NH 32
#define DH 128
#define HD 4096
#define NQKV 12288
#define MEMR 8
#define SEG 32
#define NSEG 64
#define FF 1728

typedef unsigned short u16;
typedef unsigned int u32;
typedef unsigned long long u64;
typedef __attribute__((ext_vector_type(8))) short bf16x8;
typedef __attribute__((ext_vector_type(4))) short bf16x4;
typedef __attribute__((ext_vector_type(4))) float f32x4;
typedef __attribute__((ext_vector_type(4))) unsigned int u32x4;
typedef __attribute__((ext_vector_type(2))) unsigned int u32x2;

__device__ __forceinline__ float bf2f(u16 v) { return __uint_as_float(((u32)v) << 16); }
__device__ __forceinline__ u16 f2bf(float f) { return __builtin_bit_cast(u16, (__bf16)f); }
__device__ __forceinline__ float rbf(float f) { return bf2f(f2bf(f)); }

#define MFMA16(a, b, c) __builtin_amdgcn_mfma_f32_16x16x32_bf16((a), (b), (c), 0, 0, 0)

#define WS_SYNC 0
#define WS_P_OFF 0
#define WS_MG_OFF (64 * 5120 * 4)
#define WS_ERR_OFF (WS_MG_OFF + 2560 * 8)
#define WS_SYNC_BYTES (WS_ERR_OFF + 256)
#define WS_WT 0x200000
#define WS_WOT (WS_WT + NQKV * DM * 2)
#define WS_XN (WS_WOT + DM * HD * 2)
#define WS_R (WS_XN + S_TOK * DM * 2)
#define WS_QKV (WS_R + S_TOK * 4)
#define WS_KVM (WS_QKV + (size_t)S_TOK * NQKV * 2)
#define WS_OX (WS_KVM + (size_t)NSEG * MEMR * 8192 * 2)
#define WS_IDX (WS_OX + (size_t)S_TOK * HD * 2)
#define WS_HMID (WS_IDX + 256)
#define WS_UPD (WS_HMID + 8 * FF * 4)

__global__ __launch_bounds__(256) void k_transpose(const u16* __restrict__ wq, const u16* __restrict__ wk,
                                                   const u16* __restrict__ wv, const u16* __restrict__ wo,
                                                   u16* __restrict__ wt, u16* __restrict__ wot) {
  __shared__ u16 tile[64][66];
  int b = blockIdx.x;
  const u16* src;
  u16* dst;
  int C, R, tr, tc;
  if (b < 1920) {
    int which = b / 640, rem = b % 640;
    src = which == 0 ? wq : (which == 1 ? wk : wv);
    dst = wt + (size_t)which * HD * DM;
    R = DM; C = HD;
    tr = rem / 64; tc = rem % 64;
  } else {
    int rem = b - 1920;
    src = wo; dst = wot;
    R = HD; C = DM;
    tr = rem / 10; tc = rem % 10;
  }
  int t = threadIdx.x;
#pragma unroll
  for (int i = 0; i < 2; ++i) {
    int r = (t >> 3) + 32 * i, c8 = (t & 7) * 8;
    u32x4 v = *(const u32x4*)(src + (size_t)(tr * 64 + r) * C + tc * 64 + c8);
    u16* p = &tile[r][c8];
#pragma unroll
    for (int j = 0; j < 4; ++j) { p[2 * j] = (u16)(v[j] & 0xffff); p[2 * j + 1] = (u16)(v[j] >> 16); }
  }
  __syncthreads();
#pragma unroll
  for (int i = 0; i < 2; ++i) {
    int c = (t >> 3) + 32 * i, r8 = (t & 7) * 8;
    u32x4 v;
#pragma unroll
    for (int j = 0; j < 4; ++j) v[j] = (u32)tile[r8 + 2 * j][c] | ((u32)tile[r8 + 2 * j + 1][c] << 16);
    *(u32x4*)(dst + (size_t)(tc * 64 + c) * R + tr * 64 + r8) = v;
  }
}

__global__ __launch_bounds__(256) void k_ln(const u16* __restrict__ x, const u16* __restrict__ wr,
                                            u16* __restrict__ xn, float* __restrict__ r) {
  int row = blockIdx.x * 4 + (threadIdx.x >> 6), l = threadIdx.x & 63;
  const u32* xr = (const u32*)(x + (size_t)row * DM);
  float v[10];
  float sum = 0.f;
#pragma unroll
  for (int i = 0; i < 5; ++i) {
    u32 u = xr[l + 64 * i];
    v[2 * i] = bf2f((u16)(u & 0xffff));
    v[2 * i + 1] = bf2f((u16)(u >> 16));
    sum += v[2 * i] + v[2 * i + 1];
  }
#pragma unroll
  for (int o = 32; o > 0; o >>= 1) sum += __shfl_xor(sum, o);
  float mean = sum * (1.0f / DM);
  float sq = 0.f;
#pragma unroll
  for (int i = 0; i < 10; ++i) { float d = v[i] - mean; sq += d * d; }
#pragma unroll
  for (int o = 32; o > 0; o >>= 1) sq += __shfl_xor(sq, o);
  float var = sq * (1.0f / DM);
  float mb = rbf(mean), vb = rbf(var);
  float den = rbf(sqrtf(rbf(vb + 1e-5f)));
  float acc = 0.f;
  u32* xo = (u32*)(xn + (size_t)row * DM);
  const u32* wr2 = (const u32*)wr;
#pragma unroll
  for (int i = 0; i < 5; ++i) {
    u16 a = f2bf(rbf(v[2 * i] - mb) / den);
    u16 b = f2bf(rbf(v[2 * i + 1] - mb) / den);
    xo[l + 64 * i] = (u32)a | ((u32)b << 16);
    u32 w = wr2[l + 64 * i];
    acc += bf2f(a) * bf2f((u16)(w & 0xffff)) + bf2f(b) * bf2f((u16)(w >> 16));
  }
#pragma unroll
  for (int o = 32; o > 0; o >>= 1) acc += __shfl_xor(acc, o);
  if (l == 0) r[row] = rbf(acc);
}

__global__ __launch_bounds__(256) void k_topk(const float* __restrict__ r, int* __restrict__ idx) {
  __shared__ float sv[4];
  __shared__ int si[4];
  __shared__ int win;
  int t = threadIdx.x, l = t & 63, w = t >> 6;
  float v[8];
#pragma unroll
  for (int i = 0; i < 8; ++i) v[i] = r[t + 256 * i];
  for (int k = 0; k < 8; ++k) {
    float bv = -INFINITY; int bi = 0x7fffffff;
#pragma unroll
    for (int i = 0; i < 8; ++i) {
      int id = t + 256 * i;
      if (v[i] > bv || (v[i] == bv && id < bi)) { bv = v[i]; bi = id; }
    }
#pragma unroll
    for (int o = 32; o > 0; o >>= 1) {
      float ov = __shfl_xor(bv, o); int oi = __shfl_xor(bi, o);
      if (ov > bv || (ov == bv && oi < bi)) { bv = ov; bi = oi; }
    }
    if (l == 0) { sv[w] = bv; si[w] = bi; }
    __syncthreads();
    if (t == 0) {
      float b = sv[0]; int i0 = si[0];
      for (int q = 1; q < 4; ++q) if (sv[q] > b || (sv[q] == b && si[q] < i0)) { b = sv[q]; i0 = si[q]; }
      if (i0 == 0x7fffffff) i0 = 0;
      win = i0; idx[k] = i0;
    }
    __syncthreads();
    int wi = win;
#pragma unroll
    for (int i = 0; i < 8; ++i) if (t + 256 * i == wi) v[i] = -INFINITY;
    __syncthreads();
  }
}

__global__ __launch_bounds__(1024) void k_moe1(const u16* __restrict__ xn, const int* __restrict__ idx,
                                               const u16* __restrict__ wg, const u16* __restrict__ wu,
                                               float* __restrict__ hmid) {
  __shared__ float xs[8][DM];
  __shared__ float red[16][2][8][64];
  int t = threadIdx.x, l = t & 63, w = t >> 6;
  for (int e = t; e < 8 * DM; e += 1024) {
    int m = e / DM, d = e % DM;
    xs[m][d] = bf2f(xn[(size_t)idx[m] * DM + d]);
  }
  __syncthreads();
  int col = blockIdx.x * 64 + l;
  float ag[8], au[8];
#pragma unroll
  for (int m = 0; m < 8; ++m) { ag[m] = 0.f; au[m] = 0.f; }
#pragma unroll 8
  for (int kk = 0; kk < 40; ++kk) {
    int k = w * 40 + kk;
    float g = bf2f(wg[(size_t)k * FF + col]);
    float u = bf2f(wu[(size_t)k * FF + col]);
#pragma unroll
    for (int m = 0; m < 8; ++m) { float xv = xs[m][k]; ag[m] += xv * g; au[m] += xv * u; }
  }
#pragma unroll
  for (int m = 0; m < 8; ++m) { red[w][0][m][l] = ag[m]; red[w][1][m][l] = au[m]; }
  __syncthreads();
  if (t < 512) {
    int m = t >> 6;
    float g = 0.f, u = 0.f;
#pragma unroll
    for (int q = 0; q < 16; ++q) { g += red[q][0][m][l]; u += red[q][1][m][l]; }
    g = rbf(g); u = rbf(u);
    float sg = rbf(g / (1.0f + __expf(-g)));
    hmid[m * FF + col] = rbf(sg * u);
  }
}

__global__ __launch_bounds__(1024) void k_moe2(const float* __restrict__ hmid, const int* __restrict__ idx,
                                               const float* __restrict__ r, const u16* __restrict__ wd,
                                               float* __restrict__ upd) {
  __shared__ float hs[8][FF];
  __shared__ float red[16][8][64];
  int t = threadIdx.x, l = t & 63, w = t >> 6;
  for (int e = t; e < 8 * FF; e += 1024) hs[e / FF][e % FF] = hmid[e];
  __syncthreads();
  int col = blockIdx.x * 64 + l;
  float a[8];
#pragma unroll
  for (int m = 0; m < 8; ++m) a[m] = 0.f;
#pragma unroll 4
  for (int kk = 0; kk < 108; ++kk) {
    int k = w * 108 + kk;
    float d = bf2f(wd[(size_t)k * DM + col]);
#pragma unroll
    for (int m = 0; m < 8; ++m) a[m] += hs[m][k] * d;
  }
#pragma unroll
  for (int m = 0; m < 8; ++m) red[w][m][l] = a[m];
  __syncthreads();
  if (t < 512) {
    int m = t >> 6;
    float y = 0.f;
#pragma unroll
    for (int q = 0; q < 16; ++q) y += red[q][m][l];
    y = rbf(y);
    float rv = r[idx[m]];
    float sg = rbf(1.0f / (1.0f + __expf(-rv)));
    upd[m * DM + col] = rbf(y * sg);
  }
}

template <int EPI>
__global__ __launch_bounds__(256, 2) void k_gemm(const u16* __restrict__ A, const u16* __restrict__ Bt,
                                                 u16* __restrict__ C, int N, int K,
                                                 const u16* __restrict__ x, const u16* __restrict__ xn,
                                                 const int* __restrict__ idx, const float* __restrict__ upd) {
  __shared__ __attribute__((aligned(16))) u16 shm[2 * 128 * 32];
  int nbn = N / 128;
  int brow = (blockIdx.x / nbn) * 128, bcol = (blockIdx.x % nbn) * 128;
  int wid = threadIdx.x >> 6, lane = threadIdx.x & 63, wr = wid >> 1, wc = wid & 1, fr = lane & 15, fq = lane >> 4;
  u16* SA = shm;
  u16* SB = shm + 128 * 32;
  f32x4 acc[4][4] = {};
  for (int t = 0; t < K / 32; ++t) {
#pragma unroll
    for (int i = 0; i < 2; ++i) {
      int b = threadIdx.x * 16 + i * 4096, r = b / 64, c = (b % 64) / 2;
      __builtin_amdgcn_global_load_lds((const u32*)(A + (size_t)(brow + r) * K + t * 32 + c), (u32*)((char*)SA + b), 16, 0, 0);
      __builtin_amdgcn_global_load_lds((const u32*)(Bt + (size_t)(bcol + r) * K + t * 32 + c), (u32*)((char*)SB + b), 16, 0, 0);
    }
    asm volatile("s_waitcnt vmcnt(0)" ::: "memory");
    __syncthreads();
    bf16x8 At[4], Bf[4];
#pragma unroll
    for (int m = 0; m < 4; ++m) {
      At[m] = *(const bf16x8*)((char*)SA + (wr * 64 + m * 16 + fr) * 64 + fq * 16);
      Bf[m] = *(const bf16x8*)((char*)SB + (wc * 64 + m * 16 + fr) * 64 + fq * 16);
    }
#pragma unroll
    for (int m = 0; m < 4; ++m)
#pragma unroll
      for (int n = 0; n < 4; ++n) acc[m][n] = MFMA16(Bf[n], At[m], acc[m][n]);
    __syncthreads();
  }
#pragma unroll
  for (int m = 0; m < 4; ++m) {
    int row = brow + wr * 64 + m * 16 + fr;
    int sel = -1;
    if (EPI == 1) {
#pragma unroll
      for (int q = 0; q < 8; ++q) if (idx[q] == row) sel = q;
    }
#pragma unroll
    for (int n = 0; n < 4; ++n) {
      int col = bcol + wc * 64 + n * 16 + fq * 4;
      u32x2 o;
      if (EPI == 0) {
        o[0] = (u32)f2bf(acc[m][n][0]) | ((u32)f2bf(acc[m][n][1]) << 16);
        o[1] = (u32)f2bf(acc[m][n][2]) | ((u32)f2bf(acc[m][n][3]) << 16);
      } else {
        u32x2 xv = *(const u32x2*)(x + (size_t)row * N + col);
        u32x2 nv = *(const u32x2*)(xn + (size_t)row * N + col);
        u16 res[4];
#pragma unroll
        for (int j = 0; j < 4; ++j) {
          float xx = bf2f((u16)((xv[j >> 1] >> ((j & 1) * 16)) & 0xffff));
          float nn = bf2f((u16)((nv[j >> 1] >> ((j & 1) * 16)) & 0xffff));
          float a = rbf(acc[m][n][j]);
          float bo = nn;
          if (sel >= 0) bo = rbf(nn + upd[sel * DM + col + j]);
          res[j] = f2bf(rbf(xx + a) + bo);
        }
        o[0] = (u32)res[0] | ((u32)res[1] << 16);
        o[1] = (u32)res[2] | ((u32)res[3] << 16);
      }
      *(u32x2*)(C + (size_t)row * N + col) = o;
    }
  }
}

#define MS_STRIDE 672
#define QS_STRIDE 136
#define VT_STRIDE 72
#define LDS_VW 0
#define LDS_MEM (LDS_VW + 81920)
#define LDS_Q (LDS_MEM + 10752)
#define LDS_K (LDS_Q + 2176)
#define LDS_VT (LDS_K + 13056)
#define LDS_RED (LDS_VT + 9216)
#define LDS_WO (LDS_RED + 2048)
#define LDS_TOTAL (LDS_WO + 24576)

#define SPIN_CAP (1u << 15)

__global__ __launch_bounds__(256, 1) void k_recur(const u16* __restrict__ Wt, const u16* __restrict__ Wot,
                                                  const u16* __restrict__ mem0, const u16* __restrict__ qkvx,
                                                  u16* __restrict__ kvm, float* P, u64* Mg, u32* err) {
  extern __shared__ __attribute__((aligned(16))) char smem[];
  u16* vW = (u16*)(smem + LDS_VW);
  u16* memS = (u16*)(smem + LDS_MEM);
  u16* qS = (u16*)(smem + LDS_Q);
  u16* KS = (u16*)(smem + LDS_K);
  u16* VT = (u16*)(smem + LDS_VT);
  float* red = (float*)(smem + LDS_RED);
  u16* woL = (u16*)(smem + LDS_WO);

  const int c = blockIdx.x, h = c >> 1, half = c & 1;
  const int tid = threadIdx.x, w = tid >> 6, l = tid & 63, fr = l & 15, g = l >> 4;

  __amdgpu_buffer_rsrc_t rP = __builtin_amdgcn_make_buffer_rsrc((void*)P, 0, 64 * 5120 * 4, 0x00020000);
  __amdgpu_buffer_rsrc_t rM = __builtin_amdgcn_make_buffer_rsrc((void*)Mg, 0, 2560 * 8, 0x00020000);

  bf16x8 wqk[4][20];
  bf16x8 wo[7][2];
#pragma unroll
  for (int t = 0; t < 4; ++t) {
    const u16* wp = Wt + (size_t)((t >> 1) * HD + h * DH + 32 * w + 16 * (t & 1) + fr) * DM + 8 * g;
#pragma unroll
    for (int ks = 0; ks < 20; ++ks) wqk[t][ks] = *(const bf16x8*)(wp + 32 * ks);
  }
  {
    const u16* wp = Wt + (size_t)(2 * HD + h * DH + 64 * half + 16 * w + fr) * DM + 8 * g;
#pragma unroll
    for (int ks = 0; ks < 20; ++ks) *(bf16x8*)(vW + (w * 20 + ks) * 512 + l * 8) = *(const bf16x8*)(wp + 32 * ks);
  }
#pragma unroll
  for (int T = 0; T < 10; ++T) {
    const u16* wp = Wot + (size_t)(160 * w + 16 * T + fr) * HD + h * DH + 64 * half + 4 * g;
#pragma unroll
    for (int u = 0; u < 2; ++u) {
      bf16x4 lo = *(const bf16x4*)(wp + 32 * u);
      bf16x4 hi = *(const bf16x4*)(wp + 32 * u + 16);
      bf16x8 f = __builtin_shufflevector(lo, hi, 0, 1, 2, 3, 4, 5, 6, 7);
      if (T < 7) wo[T < 7 ? T : 0][u] = f;
      else *(bf16x8*)(woL + (w * 6 + (T - 7) * 2 + u) * 512 + l * 8) = f;
    }
  }
  for (int e = tid; e < 8 * QS_STRIDE; e += 256) KS[40 * QS_STRIDE + e] = 0;
  for (int e = tid; e < 64 * 32; e += 256) VT[(e >> 5) * VT_STRIDE + 40 + (e & 31)] = 0;

  u32x4 pk0, pk1, pv;
  const int pkr = tid >> 4, pkc = (tid & 15) * 8;
  const int pvr = tid >> 3, pvc = (tid & 7) * 8;
  {
    const u16* kp = qkvx + (size_t)pkr * NQKV + HD + h * DH + pkc;
    pk0 = *(const u32x4*)kp;
    pk1 = *(const u32x4*)(kp + (size_t)16 * NQKV);
    pv = *(const u32x4*)(qkvx + (size_t)pvr * NQKV + 2 * HD + h * DH + 64 * half + pvc);
  }

  bool dead = false;
  for (int s = 0; s < NSEG; ++s) {
    if (s == 0) {
#pragma unroll
      for (int i = 0; i < 5; ++i) {
        int f = 4 * (tid + 256 * i);
        int m = f / DM, col = f % DM;
        *(u32x2*)(memS + m * MS_STRIDE + col) = *(const u32x2*)(mem0 + f);
      }
    } else {
      u32x4 v[5];
      u32 spins = 0;
      for (;;) {
        bool ok = true;
#pragma unroll
        for (int i = 0; i < 5; ++i) {
          v[i] = __builtin_amdgcn_raw_buffer_load_b128(rM, (tid + 256 * i) * 16, 0, 16);
        }
#pragma unroll
        for (int i = 0; i < 5; ++i) ok = ok && (v[i][1] == (u32)s) && (v[i][3] == (u32)s);
        if (__all(ok)) break;
        asm volatile("" ::: "memory");
        if (dead || ++spins > SPIN_CAP) { if (l == 0) atomicOr(err, 1u); dead = true; break; }
      }
#pragma unroll
      for (int i = 0; i < 5; ++i) {
        int f = 4 * (tid + 256 * i);
        int m = f / DM, col = f % DM;
        u32x2 o; o[0] = v[i][0]; o[1] = v[i][2];
        *(u32x2*)(memS + m * MS_STRIDE + col) = o;
      }
    }
    __syncthreads();

    *(u32x4*)(KS + (8 + pkr) * QS_STRIDE + pkc) = pk0;
    *(u32x4*)(KS + (24 + pkr) * QS_STRIDE + pkc) = pk1;
#pragma unroll
    for (int j = 0; j < 4; ++j) {
      VT[(pvc + 2 * j) * VT_STRIDE + 8 + pvr] = (u16)(pv[j] & 0xffff);
      VT[(pvc + 2 * j + 1) * VT_STRIDE + 8 + pvr] = (u16)(pv[j] >> 16);
    }
    if (s + 1 < NSEG) {
      const u16* kp = qkvx + (size_t)((s + 1) * SEG + pkr) * NQKV + HD + h * DH + pkc;
      pk0 = *(const u32x4*)kp;
      pk1 = *(const u32x4*)(kp + (size_t)16 * NQKV);
      pv = *(const u32x4*)(qkvx + (size_t)((s + 1) * SEG + pvr) * NQKV + 2 * HD + h * DH + 64 * half + pvc);
    }

    f32x4 acc[5] = {};
#pragma unroll
    for (int ks = 0; ks < 20; ++ks) {
      bf16x8 b = *(const bf16x8*)(memS + (fr & 7) * MS_STRIDE + 32 * ks + 8 * g);
      bf16x8 vf = *(const bf16x8*)(vW + (w * 20 + ks) * 512 + l * 8);
#pragma unroll
      for (int t = 0; t < 4; ++t) acc[t] = MFMA16(wqk[t][ks], b, acc[t]);
      acc[4] = MFMA16(vf, b, acc[4]);
    }
    if (fr < 8) {
      u32x2 o[5];
#pragma unroll
      for (int t = 0; t < 5; ++t) {
        o[t][0] = (u32)f2bf(acc[t][0]) | ((u32)f2bf(acc[t][1]) << 16);
        o[t][1] = (u32)f2bf(acc[t][2]) | ((u32)f2bf(acc[t][3]) << 16);
      }
      *(u32x2*)(qS + fr * QS_STRIDE + 32 * w + 4 * g) = o[0];
      *(u32x2*)(qS + fr * QS_STRIDE + 32 * w + 16 + 4 * g) = o[1];
      *(u32x2*)(KS + fr * QS_STRIDE + 32 * w + 4 * g) = o[2];
      *(u32x2*)(KS + fr * QS_STRIDE + 32 * w + 16 + 4 * g) = o[3];
      VT[(16 * w + 4 * g + 0) * VT_STRIDE + fr] = (u16)(o[4][0] & 0xffff);
      VT[(16 * w + 4 * g + 1) * VT_STRIDE + fr] = (u16)(o[4][0] >> 16);
      VT[(16 * w + 4 * g + 2) * VT_STRIDE + fr] = (u16)(o[4][1] & 0xffff);
      VT[(16 * w + 4 * g + 3) * VT_STRIDE + fr] = (u16)(o[4][1] >> 16);
      u16* kv = kvm + (size_t)(s * MEMR + fr) * 8192;
      if (half == 0) {
        *(u32x2*)(kv + h * DH + 32 * w + 4 * g) = o[2];
        *(u32x2*)(kv + h * DH + 32 * w + 16 + 4 * g) = o[3];
      }
      *(u32x2*)(kv + HD + h * DH + 64 * half + 16 * w + 4 * g) = o[4];
    }
    if (s + 1 == NSEG) break;
    __syncthreads();

    f32x4 sc[3] = {};
#pragma unroll
    for (int ks = 0; ks < 4; ++ks) {
      bf16x8 b = *(const bf16x8*)(qS + (fr & 7) * QS_STRIDE + 32 * ks + 8 * g);
#pragma unroll
      for (int t = 0; t < 3; ++t) {
        bf16x8 a = *(const bf16x8*)(KS + (16 * t + fr) * QS_STRIDE + 32 * ks + 8 * g);
        sc[t] = MFMA16(a, b, sc[t]);
      }
    }
    float pr[3][4];
    float mx = -INFINITY;
#pragma unroll
    for (int t = 0; t < 3; ++t)
#pragma unroll
      for (int r = 0; r < 4; ++r) {
        float v = rbf(rbf(sc[t][r]) * 0.08838834764831845f);
        if (t == 2 && g >= 2) v = -INFINITY;
        pr[t][r] = v;
        mx = fmaxf(mx, v);
      }
    mx = fmaxf(mx, __shfl_xor(mx, 16));
    mx = fmaxf(mx, __shfl_xor(mx, 32));
    float sum = 0.f;
#pragma unroll
    for (int t = 0; t < 3; ++t)
#pragma unroll
      for (int r = 0; r < 4; ++r) { float e = __expf(pr[t][r] - mx); pr[t][r] = e; sum += e; }
    sum += __shfl_xor(sum, 16);
    sum += __shfl_xor(sum, 32);
    float inv = 1.0f / sum;
    bf16x8 pb[2];
#pragma unroll
    for (int r = 0; r < 4; ++r) {
      pb[0][r] = (short)f2bf(pr[0][r] * inv);
      pb[0][4 + r] = (short)f2bf(pr[1][r] * inv);
      pb[1][r] = (short)f2bf(pr[2][r] * inv);
      pb[1][4 + r] = 0;
    }
    bf16x8 ob[2];
#pragma unroll
    for (int T = 0; T < 4; ++T) {
      f32x4 oa = {};
#pragma unroll
      for (int u = 0; u < 2; ++u) {
        bf16x4 lo = *(const bf16x4*)(VT + (16 * T + fr) * VT_STRIDE + 32 * u + 4 * g);
        bf16x4 hi = *(const bf16x4*)(VT + (16 * T + fr) * VT_STRIDE + 32 * u + 16 + 4 * g);
        bf16x8 a = __builtin_shufflevector(lo, hi, 0, 1, 2, 3, 4, 5, 6, 7);
        oa = MFMA16(a, pb[u], oa);
      }
#pragma unroll
      for (int r = 0; r < 4; ++r) ob[T >> 1][(T & 1) * 4 + r] = (short)f2bf(oa[r]);
    }
    const u32 tagp = (u32)(s % 3) + 1u;
#pragma unroll
    for (int T = 0; T < 10; ++T) {
      f32x4 pa = {};
      if (T < 7) {
        pa = MFMA16(wo[T < 7 ? T : 0][0], ob[0], pa);
        pa = MFMA16(wo[T < 7 ? T : 0][1], ob[1], pa);
      } else {
        bf16x8 f0 = *(const bf16x8*)(woL + (w * 6 + (T - 7) * 2 + 0) * 512 + l * 8);
        bf16x8 f1 = *(const bf16x8*)(woL + (w * 6 + (T - 7) * 2 + 1) * 512 + l * 8);
        pa = MFMA16(f0, ob[0], pa);
        pa = MFMA16(f1, ob[1], pa);
      }
      if (fr < 8) {
        u32x4 o;
#pragma unroll
        for (int r = 0; r < 4; ++r) o[r] = (__float_as_uint(pa[r]) & ~3u) | tagp;
        __builtin_amdgcn_raw_buffer_store_b128(o, rP, (c * 5120 + fr * DM + 160 * w + 16 * T + 4 * g) * 4, 0, 16);
      }
    }
    if (c < 40) {
      u32x4 v[8];
      u32 spins = 0;
      const int base = ((16 * w + (l >> 5)) * 5120 + c * 128 + (l & 31) * 4) * 4;
      for (;;) {
        bool ok = true;
#pragma unroll
        for (int i = 0; i < 8; ++i) v[i] = __builtin_amdgcn_raw_buffer_load_b128(rP, base + i * 2 * 5120 * 4, 0, 16);
#pragma unroll
        for (int i = 0; i < 8; ++i)
          ok = ok && ((v[i][0] & v[i][1] & v[i][2] & v[i][3] & 3u) == tagp) && (((v[i][0] | v[i][1] | v[i][2] | v[i][3]) & 3u) == tagp);
        if (__all(ok)) break;
        asm volatile("" ::: "memory");
        if (dead || ++spins > SPIN_CAP) { if (l == 0) atomicOr(err, 2u); dead = true; break; }
      }
      f32x4 a = {};
#pragma unroll
      for (int i = 0; i < 8; ++i)
#pragma unroll
        for (int r = 0; r < 4; ++r) a[r] += __uint_as_float(v[i][r] & ~3u);
#pragma unroll
      for (int r = 0; r < 4; ++r) a[r] += __shfl_xor(a[r], 32);
      if (l < 32) *(f32x4*)(red + w * 128 + l * 4) = a;
      __syncthreads();
      if (w == 0) {
        float x0 = red[2 * l] + red[128 + 2 * l] + red[256 + 2 * l] + red[384 + 2 * l];
        float x1 = red[2 * l + 1] + red[128 + 2 * l + 1] + red[256 + 2 * l + 1] + red[384 + 2 * l + 1];
        u32x2 o;
        o[0] = (u32)f2bf(x0) | ((u32)f2bf(x1) << 16);
        o[1] = (u32)(s + 1);
        __builtin_amdgcn_raw_buffer_store_b64(o, rM, (c * 64 + l) * 8, 0, 16);
      }
    }
  }
}

__global__ __launch_bounds__(256) void k_attn_x(const u16* __restrict__ qkvx, const u16* __restrict__ kvm,
                                                u16* __restrict__ ox) {
  __shared__ __attribute__((aligned(16))) u16 Vs[4][64][136];
  int w = threadIdx.x >> 6, l = threadIdx.x & 63, fr = l & 15, g = l >> 4;
  int s = blockIdx.x >> 3, h = (blockIdx.x & 7) * 4 + w;
  const u16* Qb = qkvx + (size_t)(s * SEG) * NQKV + h * DH;
  const u16* Kb = Qb + HD;
  const u16* Vb = Qb + 2 * HD;
  const u16* Km = kvm + (size_t)(s * MEMR) * 8192 + h * DH;
  const u16* Vm = Km + HD;
  for (int e = l; e < 64 * 16; e += 64) {
    int j = e >> 4, ch = (e & 15) * 8;
    u32x4 v = {0u, 0u, 0u, 0u};
    if (j < 8) v = *(const u32x4*)(Vm + (size_t)j * 8192 + ch);
    else if (j < 40) v = *(const u32x4*)(Vb + (size_t)(j - 8) * NQKV + ch);
    *(u32x4*)(&Vs[w][j][ch]) = v;
  }
  f32x4 sc[2][3] = {};
#pragma unroll
  for (int ks = 0; ks < 4; ++ks) {
    bf16x8 a[3], b[2];
#pragma unroll
    for (int t = 0; t < 3; ++t) {
      int j = 16 * t + fr;
      const u16* p = (j < 8) ? (Km + (size_t)j * 8192) : (Kb + (size_t)((j < 40 ? j : 39) - 8) * NQKV);
      a[t] = *(const bf16x8*)(p + 32 * ks + 8 * g);
    }
#pragma unroll
    for (int it = 0; it < 2; ++it) b[it] = *(const bf16x8*)(Qb + (size_t)(16 * it + fr) * NQKV + 32 * ks + 8 * g);
#pragma unroll
    for (int it = 0; it < 2; ++it)
#pragma unroll
      for (int t = 0; t < 3; ++t) sc[it][t] = MFMA16(a[t], b[it], sc[it][t]);
  }
  bf16x8 pb[2][2];
#pragma unroll
  for (int it = 0; it < 2; ++it) {
    int i = 16 * it + fr;
    float pr[3][4];
    float mx = -INFINITY;
#pragma unroll
    for (int t = 0; t < 3; ++t)
#pragma unroll
      for (int r = 0; r < 4; ++r) {
        int j = 16 * t + 4 * g + r;
        float v = rbf(rbf(sc[it][t][r]) * 0.08838834764831845f);
        if (j > 8 + i) v = -INFINITY;
        pr[t][r] = v;
        mx = fmaxf(mx, v);
      }
    mx = fmaxf(mx, __shfl_xor(mx, 16));
    mx = fmaxf(mx, __shfl_xor(mx, 32));
    float sum = 0.f;
#pragma unroll
    for (int t = 0; t < 3; ++t)
#pragma unroll
      for (int r = 0; r < 4; ++r) { float e = __expf(pr[t][r] - mx); pr[t][r] = e; sum += e; }
    sum += __shfl_xor(sum, 16);
    sum += __shfl_xor(sum, 32);
    float inv = 1.0f / sum;
#pragma unroll
    for (int r = 0; r < 4; ++r) {
      pb[it][0][r] = (short)f2bf(pr[0][r] * inv);
      pb[it][0][4 + r] = (short)f2bf(pr[1][r] * inv);
      pb[it][1][r] = (short)f2bf(pr[2][r] * inv);
      pb[it][1][4 + r] = 0;
    }
  }
  __syncthreads();
#pragma unroll
  for (int T = 0; T < 8; ++T) {
    f32x4 oa[2] = {};
#pragma unroll
    for (int u = 0; u < 2; ++u) {
      bf16x8 a;
#pragma unroll
      for (int i = 0; i < 8; ++i) a[i] = (short)Vs[w][32 * u + 16 * (i >> 2) + 4 * g + (i & 3)][16 * T + fr];
#pragma unroll
      for (int it = 0; it < 2; ++it) oa[it] = MFMA16(a, pb[it][u], oa[it]);
    }
#pragma unroll
    for (int it = 0; it < 2; ++it) {
      u32x2 o;
      o[0] = (u32)f2bf(oa[it][0]) | ((u32)f2bf(oa[it][1]) << 16);
      o[1] = (u32)f2bf(oa[it][2]) | ((u32)f2bf(oa[it][3]) << 16);
      *(u32x2*)(ox + (size_t)(s * SEG + 16 * it + fr) * HD + h * DH + 16 * T + 4 * g) = o;
    }
  }
}

extern "C" void kernel_launch(void* const* d_in, const int* in_sizes, int n_in, void* d_out, int out_size,
                              void* d_ws, size_t ws_size, hipStream_t stream) {
  const u16* x = (const u16*)d_in[0];
  const u16* mem0 = (const u16*)d_in[1];
  const u16* wq = (const u16*)d_in[2];
  const u16* wk = (const u16*)d_in[3];
  const u16* wv = (const u16*)d_in[4];
  const u16* wo = (const u16*)d_in[5];
  const u16* wr = (const u16*)d_in[8];
  const u16* wg = (const u16*)d_in[9];
  const u16* wu = (const u16*)d_in[10];
  const u16* wd = (const u16*)d_in[11];
  u16* out = (u16*)d_out;
  char* ws = (char*)d_ws;
  float* P = (float*)(ws + WS_P_OFF);
  u64* Mg = (u64*)(ws + WS_MG_OFF);
  u32* err = (u32*)(ws + WS_ERR_OFF);
  u16* Wt = (u16*)(ws + WS_WT);
  u16* Wot = (u16*)(ws + WS_WOT);
  u16* xn = (u16*)(ws + WS_XN);
  float* r = (float*)(ws + WS_R);
  u16* qkvx = (u16*)(ws + WS_QKV);
  u16* kvm = (u16*)(ws + WS_KVM);
  u16* ox = (u16*)(ws + WS_OX);
  int* idx = (int*)(ws + WS_IDX);
  float* hmid = (float*)(ws + WS_HMID);
  float* upd = (float*)(ws + WS_UPD);

  hipMemsetAsync(ws + WS_SYNC, 0, WS_SYNC_BYTES, stream);
  k_transpose<<<2560, 256, 0, stream>>>(wq, wk, wv, wo, Wt, Wot);
  k_ln<<<512, 256, 0, stream>>>(x, wr, xn, r);
  k_topk<<<1, 256, 0, stream>>>(r, idx);
  k_moe1<<<27, 1024, 0, stream>>>(xn, idx, wg, wu, hmid);
  k_moe2<<<10, 1024, 0, stream>>>(hmid, idx, r, wd, upd);
  k_gemm<0><<<16 * 96, 256, 0, stream>>>(xn, Wt, qkvx, NQKV, DM, nullptr, nullptr, nullptr, nullptr);
  hipFuncSetAttribute((const void*)k_recur, hipFuncAttributeMaxDynamicSharedMemorySize, LDS_TOTAL);
  k_recur<<<64, 256, LDS_TOTAL, stream>>>(Wt, Wot, mem0, qkvx, kvm, P, Mg, err);
  k_attn_x<<<512, 256, 0, stream>>>(qkvx, kvm, ox);
  k_gemm<1><<<16 * 5, 256, 0, stream>>>(ox, Wot, out, DM, HD, x, xn, idx, upd);
}
```

```cpp
#include <hip/hip_runtime.h>
#include <hip/hip_bf16.h>
#include <stdint.h>

#define S_TOK 2048
#define DM 640
#define NH 32
#define DH 128
#define HD 4096
#define NQKV 12288
#define MEMR 8
#define SEG 32
#define NSEG 64
#define FF 1728

typedef unsigned short u16;
typedef unsigned int u32;
typedef unsigned long long u64;
typedef __attribute__((ext_vector_type(8))) short bf16x8;
typedef __attribute__((ext_vector_type(4))) short bf16x4;
typedef __attribute__((ext_vector_type(4))) float f32x4;
typedef __attribute__((ext_vector_type(4))) unsigned int u32x4;
typedef __attribute__((ext_vector_type(2))) unsigned int u32x2;

__device__ __forceinline__ float bf2f(u16 v) { return __uint_as_float(((u32)v) << 16); }
__device__ __forceinline__ u16 f2bf(float f) { return __builtin_bit_cast(u16, (__bf16)f); }
__device__ __forceinline__ float rbf(float f) { return bf2f(f2bf(f)); }

#define ZERO4 (f32x4{0.f, 0.f, 0.f, 0.f})
#define MFMA16(a, b, c) __builtin_amdgcn_mfma_f32_16x16x32_bf16((a), (b), (c), 0, 0, 0)

#define WS_SYNC 0
#define WS_P_OFF 0
#define WS_MG_OFF (64 * 5120 * 4)
#define WS_ERR_OFF (WS_MG_OFF + 2560 * 8)
#define WS_SYNC_BYTES (WS_ERR_OFF + 256)
#define WS_WT 0x200000
#define WS_WOT (WS_WT + NQKV * DM * 2)
#define WS_XN (WS_WOT + DM * HD * 2)
#define WS_R (WS_XN + S_TOK * DM * 2)
#define WS_QKV (WS_R + S_TOK * 4)
#define WS_KVM (WS_QKV + (size_t)S_TOK * NQKV * 2)
#define WS_OX (WS_KVM + (size_t)NSEG * MEMR * 8192 * 2)
#define WS_IDX (WS_OX + (size_t)S_TOK * HD * 2)
#define WS_HMID (WS_IDX + 256)
#define WS_UPD (WS_HMID + 8 * FF * 4)

__global__ __launch_bounds__(256) void k_transpose(const u16* __restrict__ wq, const u16* __restrict__ wk,
                                                   const u16* __restrict__ wv, const u16* __restrict__ wo,
                                                   u16* __restrict__ wt, u16* __restrict__ wot) {
  __shared__ u16 tile[64][66];
  int b = blockIdx.x;
  const u16* src;
  u16* dst;
  int C, R, tr, tc;
  if (b < 1920) {
    int which = b / 640, rem = b % 640;
    src = which == 0 ? wq : (which == 1 ? wk : wv);
    dst = wt + (size_t)which * HD * DM;
    R = DM; C = HD;
    tr = rem / 64; tc = rem % 64;
  } else {
    int rem = b - 1920;
    src = wo; dst = wot;
    R = HD; C = DM;
    tr = rem / 10; tc = rem % 10;
  }
  int t = threadIdx.x;
#pragma unroll
  for (int i = 0; i < 2; ++i) {
    int r = (t >> 3) + 32 * i, c8 = (t & 7) * 8;
    u32x4 v = *(const u32x4*)(src + (size_t)(tr * 64 + r) * C + tc * 64 + c8);
    u16* p = &tile[r][c8];
#pragma unroll
    for (int j = 0; j < 4; ++j) { p[2 * j] = (u16)(v[j] & 0xffff); p[2 * j + 1] = (u16)(v[j] >> 16); }
  }
  __syncthreads();
#pragma unroll
  for (int i = 0; i < 2; ++i) {
    int c = (t >> 3) + 32 * i, r8 = (t & 7) * 8;
    u32x4 v;
#pragma unroll
    for (int j = 0; j < 4; ++j) v[j] = (u32)tile[r8 + 2 * j][c] | ((u32)tile[r8 + 2 * j + 1][c] << 16);
    *(u32x4*)(dst + (size_t)(tc * 64 + c) * R + tr * 64 + r8) = v;
  }
}

__global__ __launch_bounds__(256) void k_ln(const u16* __restrict__ x, const u16* __restrict__ wr,
                                            u16* __restrict__ xn, float* __restrict__ r) {
  int row = blockIdx.x * 4 + (threadIdx.x >> 6), l = threadIdx.x & 63;
  const u32* xr = (const u32*)(x + (size_t)row * DM);
  float v[10];
  float sum = 0.f;
#pragma unroll
  for (int i = 0; i < 5; ++i) {
    u32 u = xr[l + 64 * i];
    v[2 * i] = bf2f((u16)(u & 0xffff));
    v[2 * i + 1] = bf2f((u16)(u >> 16));
    sum += v[2 * i] + v[2 * i + 1];
  }
#pragma unroll
  for (int o = 32; o > 0; o >>= 1) sum += __shfl_xor(sum, o);
  float mean = sum * (1.0f / DM);
  float sq = 0.f;
#pragma unroll
  for (int i = 0; i < 10; ++i) { float d = v[i] - mean; sq += d * d; }
#pragma unroll
  for (int o = 32; o > 0; o >>= 1) sq += __shfl_xor(sq, o);
  float var = sq * (1.0f / DM);
  float mb = rbf(mean), vb = rbf(var);
  float den = rbf(sqrtf(rbf(vb + 1e-5f)));
  float acc = 0.f;
  u32* xo = (u32*)(xn + (size_t)row * DM);
  const u32* wr2 = (const u32*)wr;
#pragma unroll
  for (int i = 0; i < 5; ++i) {
    u16 a = f2bf(rbf(v[2 * i] - mb) / den);
    u16 b = f2bf(rbf(v[2 * i + 1] - mb) / den);
    xo[l + 64 * i] = (u32)a | ((u32)b << 16);
    u32 w = wr2[l + 64 * i];
    acc += bf2f(a) * bf2f((u16)(w & 0xffff)) + bf2f(b) * bf2f((u16)(w >> 16));
  }
#pragma unroll
  for (int o = 32; o > 0; o >>= 1) acc += __shfl_xor(acc, o);
  if (l == 0) r[row] = rbf(acc);
}

__global__ __launch_bounds__(256) void k_topk(const float* __restrict__ r, int* __restrict__ idx) {
  __shared__ float sv[4];
  __shared__ int si[4];
  __shared__ int win;
  int t = threadIdx.x, l = t & 63, w = t >> 6;
  float v[8];
#pragma unroll
  for (int i = 0; i < 8; ++i) v[i] = r[t + 256 * i];
  for (int k = 0; k < 8; ++k) {
    float bv = -INFINITY; int bi = 0x7fffffff;
#pragma unroll
    for (int i = 0; i < 8; ++i) {
      int id = t + 256 * i;
      if (v[i] > bv || (v[i] == bv && id < bi)) { bv = v[i]; bi = id; }
    }
#pragma unroll
    for (int o = 32; o > 0; o >>= 1) {
      float ov = __shfl_xor(bv, o); int oi = __shfl_xor(bi, o);
      if (ov > bv || (ov == bv && oi < bi)) { bv = ov; bi = oi; }
    }
    if (l == 0) { sv[w] = bv; si[w] = bi; }
    __syncthreads();
    if (t == 0) {
      float b = sv[0]; int i0 = si[0];
      for (int q = 1; q < 4; ++q) if (sv[q] > b || (sv[q] == b && si[q] < i0)) { b = sv[q]; i0 = si[q]; }
      if (i0 == 0x7fffffff) i0 = 0;
      win = i0; idx[k] = i0;
    }
    __syncthreads();
    int wi = win;
#pragma unroll
    for (int i = 0; i < 8; ++i) if (t + 256 * i == wi) v[i] = -INFINITY;
    __syncthreads();
  }
}

__global__ __launch_bounds__(1024) void k_moe1(const u16* __restrict__ xn, const int* __restrict__ idx,
                                               const u16* __restrict__ wg, const u16* __restrict__ wu,
                                               float* __restrict__ hmid) {
  __shared__ float xs[8][DM];
  __shared__ float red[16][2][8][64];
  int t = threadIdx.x, l = t & 63, w = t >> 6;
  for (int e = t; e < 8 * DM; e += 1024) {
    int m = e / DM, d = e % DM;
    xs[m][d] = bf2f(xn[(size_t)idx[m] * DM + d]);
  }
  __syncthreads();
  int col = blockIdx.x * 64 + l;
  float ag[8], au[8];
#pragma unroll
  for (int m = 0; m < 8; ++m) { ag[m] = 0.f; au[m] = 0.f; }
#pragma unroll 8
  for (int kk = 0; kk < 40; ++kk) {
    int k = w * 40 + kk;
    float g = bf2f(wg[(size_t)k * FF + col]);
    float u = bf2f(wu[(size_t)k * FF + col]);
#pragma unroll
    for (int m = 0; m < 8; ++m) { float xv = xs[m][k]; ag[m] += xv * g; au[m] += xv * u; }
  }
#pragma unroll
  for (int m = 0; m < 8; ++m) { red[w][0][m][l] = ag[m]; red[w][1][m][l] = au[m]; }
  __syncthreads();
  if (t < 512) {
    int m = t >> 6;
    float g = 0.f, u = 0.f;
#pragma unroll
    for (int q = 0; q < 16; ++q) { g += red[q][0][m][l]; u += red[q][1][m][l]; }
    g = rbf(g); u = rbf(u);
    float sg = rbf(g / (1.0f + __expf(-g)));
    hmid[m * FF + col] = rbf(sg * u);
  }
}

__global__ __launch_bounds__(1024) void k_moe2(const float* __restrict__ hmid, const int* __restrict__ idx,
                                               const float* __restrict__ r, const u16* __restrict__ wd,
                                               float* __restrict__ upd) {
  __shared__ float hs[8][FF];
  __shared__ float red[16][8][64];
  int t = threadIdx.x, l = t & 63, w = t >> 6;
  for (int e = t; e < 8 * FF; e += 1024) hs[e / FF][e % FF] = hmid[e];
  __syncthreads();
  int col = blockIdx.x * 64 + l;
  float a[8];
#pragma unroll
  for (int m = 0; m < 8; ++m) a[m] = 0.f;
#pragma unroll 4
  for (int kk = 0; kk < 108; ++kk) {
    int k = w * 108 + kk;
    float d = bf2f(wd[(size_t)k * DM + col]);
#pragma unroll
    for (int m = 0; m < 8; ++m) a[m] += hs[m][k] * d;
  }
#pragma unroll
  for (int m = 0; m < 8; ++m) red[w][m][l] = a[m];
  __syncthreads();
  if (t < 512) {
    int m = t >> 6;
    float y = 0.f;
#pragma unroll
    for (int q = 0; q < 16; ++q) y += red[q][m][l];
    y = rbf(y);
    float rv = r[idx[m]];
    float sg = rbf(1.0f / (1.0f + __expf(-rv)));
    upd[m * DM + col] = rbf(y * sg);
  }
}

template <int EPI>
__global__ __launch_bounds__(256, 2) void k_gemm(const u16* __restrict__ A, const u16* __restrict__ Bt,
                                                 u16* __restrict__ C, int N, int K,
                                                 const u16* __restrict__ x, const u16* __restrict__ xn,
                                                 const int* __restrict__ idx, const float* __restrict__ upd) {
  __shared__ __attribute__((aligned(16))) u16 shm[2 * 128 * 32];
  int nbn = N / 128;
  int brow = (blockIdx.x / nbn) * 128, bcol = (blockIdx.x % nbn) * 128;
  int wid = threadIdx.x >> 6, lane = threadIdx.x & 63, wr = wid >> 1, wc = wid & 1, fr = lane & 15, fq = lane >> 4;
  u16* SA = shm;
  u16* SB = shm + 128 * 32;
  f32x4 acc[4][4] = {};
  for (int t = 0; t < K / 32; ++t) {
#pragma unroll
    for (int i = 0; i < 2; ++i) {
      int b = threadIdx.x * 16 + i * 4096, r = b / 64, c = (b % 64) / 2;
      __builtin_amdgcn_global_load_lds((const u32*)(A + (size_t)(brow + r) * K + t * 32 + c), (u32*)((char*)SA + b), 16, 0, 0);
      __builtin_amdgcn_global_load_lds((const u32*)(Bt + (size_t)(bcol + r) * K + t * 32 + c), (u32*)((char*)SB + b), 16, 0, 0);
    }
    asm volatile("s_waitcnt vmcnt(0)" ::: "memory");
    __syncthreads();
    bf16x8 At[4], Bf[4];
#pragma unroll
    for (int m = 0; m < 4; ++m) {
      At[m] = *(const bf16x8*)((char*)SA + (wr * 64 + m * 16 + fr) * 64 + fq * 16);
      Bf[m] = *(const bf16x8*)((char*)SB + (wc * 64 + m * 16 + fr) * 64 + fq * 16);
    }
#pragma unroll
    for (int m = 0; m < 4; ++m)
#pragma unroll
      for (int n = 0; n < 4; ++n) acc[m][n] = MFMA16(Bf[n], At[m], acc[m][n]);
    __syncthreads();
  }
#pragma unroll
  for (int m = 0; m < 4; ++m) {
    int row = brow + wr * 64 + m * 16 + fr;
    int sel = -1;
    if (EPI == 1) {
#pragma unroll
      for (int q = 0; q < 8; ++q) if (idx[q] == row) sel = q;
    }
#pragma unroll
    for (int n = 0; n < 4; ++n) {
      int col = bcol + wc * 64 + n * 16 + fq * 4;
      u32x2 o;
      if (EPI == 0) {
        o[0] = (u32)f2bf(acc[m][n][0]) | ((u32)f2bf(acc[m][n][1]) << 16);
        o[1] = (u32)f2bf(acc[m][n][2]) | ((u32)f2bf(acc[m][n][3]) << 16);
      } else {
        u32x2 xv = *(const u32x2*)(x + (size_t)row * N + col);
        u32x2 nv = *(const u32x2*)(xn + (size_t)row * N + col);
        u16 res[4];
#pragma unroll
        for (int j = 0; j < 4; ++j) {
          float xx = bf2f((u16)((xv[j >> 1] >> ((j & 1) * 16)) & 0xffff));
          float nn = bf2f((u16)((nv[j >> 1] >> ((j & 1) * 16)) & 0xffff));
          float a = rbf(acc[m][n][j]);
          float bo = nn;
          if (sel >= 0) bo = rbf(nn + upd[sel * DM + col + j]);
          res[j] = f2bf(rbf(xx + a) + bo);
        }
        o[0] = (u32)res[0] | ((u32)res[1] << 16);
        o[1] = (u32)res[2] | ((u32)res[3] << 16);
      }
      *(u32x2*)(C + (size_t)row * N + col) = o;
    }
  }
}

#define MS_STRIDE 672
#define QS_STRIDE 136
#define VT_STRIDE 72
#define KS_ELEMS (48 * QS_STRIDE)
#define VT_ELEMS (64 * VT_STRIDE)
#define LDS_VW 0
#define LDS_MEM (LDS_VW + 81920)
#define LDS_Q (LDS_MEM + 10752)
#define LDS_K (LDS_Q + 2176)
#define LDS_VT (LDS_K + 26112)
#define LDS_WO (LDS_VT + 18432)
#define LDS_TOTAL (LDS_WO + 16384)

#define SPIN_CAP (1u << 15)

#define MFMA_A0(acc, wa, b) asm("v_mfma_f32_16x16x32_bf16 %0, %1, %2, 0" : "=&v"(acc) : "a"(wa), "v"(b))
#define MFMA_A(acc, wa, b) asm("v_mfma_f32_16x16x32_bf16 %0, %1, %2, %0" : "+v"(acc) : "a"(wa), "v"(b))
#define MFMA_V0(acc, wa, b) asm("v_mfma_f32_16x16x32_bf16 %0, %1, %2, 0" : "=&v"(acc) : "v"(wa), "v"(b))
#define MFMA_V(acc, wa, b) asm("v_mfma_f32_16x16x32_bf16 %0, %1, %2, %0" : "+v"(acc) : "v"(wa), "v"(b))

__device__ __forceinline__ float red_max_rows(float x) {
  u32 u = __float_as_uint(x);
  auto a = __builtin_amdgcn_permlane16_swap(u, u, false, false);
  float m = fmaxf(__uint_as_float(a[0]), __uint_as_float(a[1]));
  u = __float_as_uint(m);
  auto b = __builtin_amdgcn_permlane32_swap(u, u, false, false);
  return fmaxf(__uint_as_float(b[0]), __uint_as_float(b[1]));
}
__device__ __forceinline__ float red_sum_rows(float x) {
  u32 u = __float_as_uint(x);
  auto a = __builtin_amdgcn_permlane16_swap(u, u, false, false);
  float m = __uint_as_float(a[0]) + __uint_as_float(a[1]);
  u = __float_as_uint(m);
  auto b = __builtin_amdgcn_permlane32_swap(u, u, false, false);
  return __uint_as_float(b[0]) + __uint_as_float(b[1]);
}
__device__ __forceinline__ u32 pack2(float a, float b) {
  u32 r;
  asm("v_cvt_pk_bf16_f32 %0, %1, %2" : "=v"(r) : "v"(a), "v"(b));
  return r;
}

__global__ __launch_bounds__(256, 1) void k_recur(const u16* __restrict__ Wt, const u16* __restrict__ Wot,
                                                  const u16* __restrict__ mem0, const u16* __restrict__ qkvx,
                                                  u16* __restrict__ kvm, float* P, u64* Mg, u32* err) {
  extern __shared__ __attribute__((aligned(16))) char smem[];
  u16* vW = (u16*)(smem + LDS_VW);
  u16* memS = (u16*)(smem + LDS_MEM);
  u16* qS = (u16*)(smem + LDS_Q);
  u16* KSb = (u16*)(smem + LDS_K);
  u16* VTb = (u16*)(smem + LDS_VT);
  u16* woL = (u16*)(smem + LDS_WO);

  const int c = blockIdx.x, h = c >> 1, half = c & 1;

  __amdgpu_buffer_rsrc_t rP = __builtin_amdgcn_make_buffer_rsrc((void*)P, 0, 64 * 5120 * 4, 0x00020000);
  __amdgpu_buffer_rsrc_t rM = __builtin_amdgcn_make_buffer_rsrc((void*)Mg, 0, 2560 * 8, 0x00020000);

  bf16x8 wa[3][20];
  bf16x8 wv3[20];
  bf16x8 woa[2][2];
  bf16x8 wov[6][2];
  u32x4 pk0, pk1, pv;
  {
    const int tid = threadIdx.x, w = tid >> 6, l = tid & 63, fr = l & 15, g = l >> 4;
#pragma unroll
    for (int t = 0; t < 4; ++t) {
      const u16* wp = Wt + (size_t)((t >> 1) * HD + h * DH + 32 * w + 16 * (t & 1) + fr) * DM + 8 * g;
#pragma unroll
      for (int ks = 0; ks < 20; ++ks) {
        bf16x8 f = *(const bf16x8*)(wp + 32 * ks);
        if (t < 3) wa[t < 3 ? t : 0][ks] = f; else wv3[ks] = f;
      }
    }
    {
      const u16* wp = Wt + (size_t)(2 * HD + h * DH + 64 * half + 16 * w + fr) * DM + 8 * g;
#pragma unroll
      for (int ks = 0; ks < 20; ++ks) *(bf16x8*)(vW + (w * 20 + ks) * 512 + l * 8) = *(const bf16x8*)(wp + 32 * ks);
    }
#pragma unroll
    for (int T = 0; T < 10; ++T) {
      const u16* wp = Wot + (size_t)(160 * w + 16 * T + fr) * HD + h * DH + 64 * half + 4 * g;
#pragma unroll
      for (int u = 0; u < 2; ++u) {
        bf16x4 lo = *(const bf16x4*)(wp + 32 * u);
        bf16x4 hi = *(const bf16x4*)(wp + 32 * u + 16);
        bf16x8 f = __builtin_shufflevector(lo, hi, 0, 1, 2, 3, 4, 5, 6, 7);
        if (T < 2) woa[T < 2 ? T : 0][u] = f;
        else if (T < 8) wov[(T >= 2 && T < 8) ? T - 2 : 0][u] = f;
        else *(bf16x8*)(woL + (w * 4 + (T - 8) * 2 + u) * 512 + l * 8) = f;
      }
    }
    for (int e = tid; e < 8 * QS_STRIDE; e += 256) { KSb[40 * QS_STRIDE + e] = 0; KSb[KS_ELEMS + 40 * QS_STRIDE + e] = 0; }
    for (int e = tid; e < 64 * 32; e += 256) {
      VTb[(e >> 5) * VT_STRIDE + 40 + (e & 31)] = 0;
      VTb[VT_ELEMS + (e >> 5) * VT_STRIDE + 40 + (e & 31)] = 0;
    }
    const int pkr = tid >> 4, pkc = (tid & 15) * 8, pvr = tid >> 3, pvc = (tid & 7) * 8;
    const u16* kp = qkvx + (size_t)pkr * NQKV + HD + h * DH + pkc;
    pk0 = *(const u32x4*)kp;
    pk1 = *(const u32x4*)(kp + (size_t)16 * NQKV);
    pv = *(const u32x4*)(qkvx + (size_t)pvr * NQKV + 2 * HD + h * DH + 64 * half + pvc);
    *(u32x4*)(KSb + (8 + pkr) * QS_STRIDE + pkc) = pk0;
    *(u32x4*)(KSb + (24 + pkr) * QS_STRIDE + pkc) = pk1;
#pragma unroll
    for (int j = 0; j < 4; ++j) {
      VTb[(pvc + 2 * j) * VT_STRIDE + 8 + pvr] = (u16)(pv[j] & 0xffff);
      VTb[(pvc + 2 * j + 1) * VT_STRIDE + 8 + pvr] = (u16)(pv[j] >> 16);
    }
  }

  bool dead = false;
  for (int s = 0; s < NSEG; ++s) {
    int tz = threadIdx.x;
    asm volatile("" : "+v"(tz));
    const int tid = tz, w = tz >> 6, l = tz & 63, fr = l & 15, g = l >> 4;
    const int pkr = tid >> 4, pkc = (tid & 15) * 8;
    const int pvr = tid >> 3, pvc = (tid & 7) * 8;
    u16* KS = KSb + (s & 1) * KS_ELEMS;
    u16* VT = VTb + (s & 1) * VT_ELEMS;
    if (s == 0) {
#pragma unroll
      for (int i = 0; i < 5; ++i) {
        int f = 4 * (tid + 256 * i);
        int m = f / DM, col = f % DM;
        *(u32x2*)(memS + m * MS_STRIDE + col) = *(const u32x2*)(mem0 + f);
      }
    } else {
      u32x4 v[5];
      u32 spins = 0;
#pragma unroll
      for (int i = 0; i < 5; ++i) v[i] = __builtin_amdgcn_raw_buffer_load_b128(rM, (tid + 256 * i) * 16, 0, 16);
      for (;;) {
        bool anybad = false;
#pragma unroll
        for (int i = 0; i < 5; ++i) {
          u32 bad = (v[i][1] ^ (u32)s) | (v[i][3] ^ (u32)s);
          if (__any(bad != 0)) {
            anybad = true;
            asm volatile("" ::: "memory");
            v[i] = __builtin_amdgcn_raw_buffer_load_b128(rM, (tid + 256 * i) * 16, 0, 16);
          }
        }
        if (!anybad) break;
        if (dead || ++spins > SPIN_CAP) { if (l == 0) atomicOr(err, 1u); dead = true; break; }
      }
#pragma unroll
      for (int i = 0; i < 5; ++i) {
        int f = 4 * (tid + 256 * i);
        int m = f / DM, col = f % DM;
        u32x2 o; o[0] = v[i][0]; o[1] = v[i][2];
        *(u32x2*)(memS + m * MS_STRIDE + col) = o;
      }
    }
    __syncthreads();

    if (s + 1 < NSEG) {
      const u16* kp = qkvx + (size_t)((s + 1) * SEG + pkr) * NQKV + HD + h * DH + pkc;
      pk0 = *(const u32x4*)kp;
      pk1 = *(const u32x4*)(kp + (size_t)16 * NQKV);
      pv = *(const u32x4*)(qkvx + (size_t)((s + 1) * SEG + pvr) * NQKV + 2 * HD + h * DH + 64 * half + pvc);
    }

    f32x4 acc[5];
    {
      bf16x8 b[3][2], vf[3][2];
      const u16* mp = memS + (fr & 7) * MS_STRIDE + 8 * g;
      const u16* vp = vW + (w * 20) * 512 + l * 8;
#pragma unroll
      for (int i = 0; i < 4; ++i) { b[i >> 1][i & 1] = *(const bf16x8*)(mp + 32 * i); vf[i >> 1][i & 1] = *(const bf16x8*)(vp + 512 * i); }
#pragma unroll
      for (int ch = 0; ch < 10; ++ch) {
        if (ch < 8) {
#pragma unroll
          for (int i = 0; i < 2; ++i) {
            b[(ch + 2) % 3][i] = *(const bf16x8*)(mp + 32 * (2 * ch + 4 + i));
            vf[(ch + 2) % 3][i] = *(const bf16x8*)(vp + 512 * (2 * ch + 4 + i));
          }
        }
        __builtin_amdgcn_sched_barrier(0);
#pragma unroll
        for (int i = 0; i < 2; ++i) {
          const int ks = 2 * ch + i;
          if (ks == 0) {
            MFMA_A0(acc[0], wa[0][0], b[0][0]);
            MFMA_A0(acc[1], wa[1][0], b[0][0]);
            MFMA_A0(acc[2], wa[2][0], b[0][0]);
            MFMA_V0(acc[3], wv3[0], b[0][0]);
            MFMA_V0(acc[4], vf[0][0], b[0][0]);
          } else {
            MFMA_A(acc[0], wa[0][ks], b[ch % 3][i]);
            MFMA_A(acc[1], wa[1][ks], b[ch % 3][i]);
            MFMA_A(acc[2], wa[2][ks], b[ch % 3][i]);
            MFMA_V(acc[3], wv3[ks], b[ch % 3][i]);
            MFMA_V(acc[4], vf[ch % 3][i], b[ch % 3][i]);
          }
        }
        __builtin_amdgcn_sched_barrier(0);
      }
      asm volatile("s_nop 7\n\ts_nop 7" : "+v"(acc[0]), "+v"(acc[1]), "+v"(acc[2]), "+v"(acc[3]), "+v"(acc[4]));
    }
    u32x2 o[5];
#pragma unroll
    for (int t = 0; t < 5; ++t) { o[t][0] = pack2(acc[t][0], acc[t][1]); o[t][1] = pack2(acc[t][2], acc[t][3]); }
    if (fr < 8) {
      *(u32x2*)(qS + fr * QS_STRIDE + 32 * w + 4 * g) = o[0];
      *(u32x2*)(qS + fr * QS_STRIDE + 32 * w + 16 + 4 * g) = o[1];
      *(u32x2*)(KS + fr * QS_STRIDE + 32 * w + 4 * g) = o[2];
      *(u32x2*)(KS + fr * QS_STRIDE + 32 * w + 16 + 4 * g) = o[3];
      VT[(16 * w + 4 * g + 0) * VT_STRIDE + fr] = (u16)(o[4][0] & 0xffff);
      VT[(16 * w + 4 * g + 1) * VT_STRIDE + fr] = (u16)(o[4][0] >> 16);
      VT[(16 * w + 4 * g + 2) * VT_STRIDE + fr] = (u16)(o[4][1] & 0xffff);
      VT[(16 * w + 4 * g + 3) * VT_STRIDE + fr] = (u16)(o[4][1] >> 16);
    }
    if (s + 1 < NSEG) {
      __syncthreads();

      f32x4 sc[3];
      bf16x8 va[4][2];
      {
        bf16x8 qb[4], ka[3][4];
#pragma unroll
        for (int ks = 0; ks < 4; ++ks) {
          qb[ks] = *(const bf16x8*)(qS + (fr & 7) * QS_STRIDE + 32 * ks + 8 * g);
#pragma unroll
          for (int t = 0; t < 3; ++t) ka[t][ks] = *(const bf16x8*)(KS + (16 * t + fr) * QS_STRIDE + 32 * ks + 8 * g);
        }
#pragma unroll
        for (int t = 0; t < 3; ++t) MFMA_V0(sc[t], ka[t][0], qb[0]);
#pragma unroll
        for (int ks = 1; ks < 4; ++ks)
#pragma unroll
          for (int t = 0; t < 3; ++t) MFMA_V(sc[t], ka[t][ks], qb[ks]);
        __builtin_amdgcn_sched_barrier(0);
#pragma unroll
        for (int T = 0; T < 4; ++T)
#pragma unroll
          for (int u = 0; u < 2; ++u) {
            bf16x4 lo = *(const bf16x4*)(VT + (16 * T + fr) * VT_STRIDE + 32 * u + 4 * g);
            bf16x4 hi = *(const bf16x4*)(VT + (16 * T + fr) * VT_STRIDE + 32 * u + 16 + 4 * g);
            va[T][u] = __builtin_shufflevector(lo, hi, 0, 1, 2, 3, 4, 5, 6, 7);
          }
        asm volatile("s_nop 7\n\ts_nop 7" : "+v"(sc[0]), "+v"(sc[1]), "+v"(sc[2]));
      }
      float pr[12];
#pragma unroll
      for (int t = 0; t < 3; ++t)
#pragma unroll
        for (int r2 = 0; r2 < 2; ++r2) {
          u32 p1 = pack2(sc[t][2 * r2], sc[t][2 * r2 + 1]);
          float a0 = __uint_as_float(p1 << 16) * 0.08838834764831845f;
          float a1 = __uint_as_float(p1 & 0xffff0000u) * 0.08838834764831845f;
          u32 p2 = pack2(a0, a1);
          pr[4 * t + 2 * r2] = __uint_as_float(p2 << 16);
          pr[4 * t + 2 * r2 + 1] = __uint_as_float(p2 & 0xffff0000u);
        }
      if (g >= 2) { pr[8] = -INFINITY; pr[9] = -INFINITY; pr[10] = -INFINITY; pr[11] = -INFINITY; }
      float mx = fmaxf(fmaxf(fmaxf(pr[0], pr[1]), fmaxf(pr[2], pr[3])), fmaxf(fmaxf(pr[4], pr[5]), fmaxf(pr[6], pr[7])));
      mx = fmaxf(mx, fmaxf(fmaxf(pr[8], pr[9]), fmaxf(pr[10], pr[11])));
      mx = red_max_rows(mx);
      const float nmx = -mx * 1.4426950408889634f;
      float sum = 0.f;
#pragma unroll
      for (int i = 0; i < 12; ++i) { pr[i] = __builtin_amdgcn_exp2f(fmaf(pr[i], 1.4426950408889634f, nmx)); }
      sum = ((pr[0] + pr[1]) + (pr[2] + pr[3])) + ((pr[4] + pr[5]) + (pr[6] + pr[7])) + ((pr[8] + pr[9]) + (pr[10] + pr[11]));
      sum = red_sum_rows(sum);
      float inv = __builtin_amdgcn_rcpf(sum);
      bf16x8 pb[2];
      {
        u32x4 t0, t1;
        t0[0] = pack2(pr[0] * inv, pr[1] * inv); t0[1] = pack2(pr[2] * inv, pr[3] * inv);
        t0[2] = pack2(pr[4] * inv, pr[5] * inv); t0[3] = pack2(pr[6] * inv, pr[7] * inv);
        t1[0] = pack2(pr[8] * inv, pr[9] * inv); t1[1] = pack2(pr[10] * inv, pr[11] * inv);
        t1[2] = 0; t1[3] = 0;
        pb[0] = __builtin_bit_cast(bf16x8, t0); pb[1] = __builtin_bit_cast(bf16x8, t1);
      }
      bf16x8 ob[2];
      bf16x8 wl[4];
      {
        f32x4 oa[4];
        asm volatile("s_nop 1" : "+v"(pb[0]), "+v"(pb[1]));
#pragma unroll
        for (int T = 0; T < 4; ++T) MFMA_V0(oa[T], va[T][0], pb[0]);
#pragma unroll
        for (int T = 0; T < 4; ++T) MFMA_V(oa[T], va[T][1], pb[1]);
        __builtin_amdgcn_sched_barrier(0);
#pragma unroll
        for (int i = 0; i < 4; ++i) wl[i] = *(const bf16x8*)(woL + (w * 4 + i) * 512 + l * 8);
        asm volatile("s_nop 7\n\ts_nop 7" : "+v"(oa[0]), "+v"(oa[1]), "+v"(oa[2]), "+v"(oa[3]));
        u32x4 t0, t1;
        t0[0] = pack2(oa[0][0], oa[0][1]); t0[1] = pack2(oa[0][2], oa[0][3]);
        t0[2] = pack2(oa[1][0], oa[1][1]); t0[3] = pack2(oa[1][2], oa[1][3]);
        t1[0] = pack2(oa[2][0], oa[2][1]); t1[1] = pack2(oa[2][2], oa[2][3]);
        t1[2] = pack2(oa[3][0], oa[3][1]); t1[3] = pack2(oa[3][2], oa[3][3]);
        ob[0] = __builtin_bit_cast(bf16x8, t0); ob[1] = __builtin_bit_cast(bf16x8, t1);
      }
      const u32 tagp = (u32)(s % 3) + 1u;
      {
        f32x4 pa[10];
        asm volatile("s_nop 1" : "+v"(ob[0]), "+v"(ob[1]));
        MFMA_A0(pa[0], woa[0][0], ob[0]);
        MFMA_A0(pa[1], woa[1][0], ob[0]);
#pragma unroll
        for (int T = 2; T < 8; ++T) MFMA_V0(pa[T], wov[T - 2][0], ob[0]);
        MFMA_V0(pa[8], wl[0], ob[0]);
        MFMA_V0(pa[9], wl[2], ob[0]);
        MFMA_A(pa[0], woa[0][1], ob[1]);
        MFMA_A(pa[1], woa[1][1], ob[1]);
#pragma unroll
        for (int T = 2; T < 8; ++T) MFMA_V(pa[T], wov[T - 2][1], ob[1]);
        MFMA_V(pa[8], wl[1], ob[1]);
        MFMA_V(pa[9], wl[3], ob[1]);
        asm volatile("s_nop 7\n\ts_nop 7" : "+v"(pa[0]), "+v"(pa[1]), "+v"(pa[2]), "+v"(pa[3]), "+v"(pa[4]),
                     "+v"(pa[5]), "+v"(pa[6]), "+v"(pa[7]), "+v"(pa[8]), "+v"(pa[9]));
        const int soff = (c * 5120 + (fr & 7) * DM + 160 * w + 16 * (fr >> 3) + 4 * g) * 4;
#pragma unroll
        for (int T = 0; T < 10; T += 2) {
          u32x4 ov;
#pragma unroll
          for (int r = 0; r < 4; ++r) {
            u32 lo = __float_as_uint(pa[T][r]);
            u32 hi = __builtin_amdgcn_update_dpp(0u, __float_as_uint(pa[T + 1][r]), 0x118, 0xf, 0xf, false);
            u32 v = (fr < 8) ? lo : hi;
            ov[r] = (v & ~3u) | tagp;
          }
          __builtin_amdgcn_raw_buffer_store_b128(ov, rP, soff + 64 * T, 0, 16);
        }
      }
      if (c < 40) {
        u32x4 v[8];
        u32 spins = 0;
        const int base = ((l >> 3) * 5120 + (4 * c + w) * 32 + (l & 7) * 4) * 4;
#pragma unroll
        for (int i = 0; i < 8; ++i) v[i] = __builtin_amdgcn_raw_buffer_load_b128(rP, base + i * 8 * 5120 * 4, 0, 16);
        for (;;) {
          bool anybad = false;
#pragma unroll
          for (int i = 0; i < 8; ++i) {
            u32 bad = ((v[i][0] ^ tagp) | (v[i][1] ^ tagp) | (v[i][2] ^ tagp) | (v[i][3] ^ tagp)) & 3u;
            if (__any(bad != 0)) {
              anybad = true;
              asm volatile("" ::: "memory");
              v[i] = __builtin_amdgcn_raw_buffer_load_b128(rP, base + i * 8 * 5120 * 4, 0, 16);
            }
          }
          if (!anybad) break;
          if (dead || ++spins > SPIN_CAP) { if (l == 0) atomicOr(err, 2u); dead = true; break; }
        }
        f32x4 a;
#pragma unroll
        for (int r = 0; r < 4; ++r)
          a[r] = ((__uint_as_float(v[0][r]) + __uint_as_float(v[1][r])) + (__uint_as_float(v[2][r]) + __uint_as_float(v[3][r]))) +
                 ((__uint_as_float(v[4][r]) + __uint_as_float(v[5][r])) + (__uint_as_float(v[6][r]) + __uint_as_float(v[7][r])));
#pragma unroll
        for (int r = 0; r < 4; ++r) {
          float x = a[r];
          x += __uint_as_float(__builtin_amdgcn_update_dpp(0u, __float_as_uint(x), 0x128, 0xf, 0xf, false));
          a[r] = red_sum_rows(x);
        }
        if (l < 8) {
          u32x4 og;
          og[0] = pack2(a[0], a[1]); og[1] = (u32)(s + 1);
          og[2] = pack2(a[2], a[3]); og[3] = (u32)(s + 1);
          __builtin_amdgcn_raw_buffer_store_b128(og, rM, ((4 * c + w) * 16 + l * 2) * 8, 0, 16);
        }
      }
      {
        u16* KN = KSb + ((s + 1) & 1) * KS_ELEMS;
        u16* VN = VTb + ((s + 1) & 1) * VT_ELEMS;
        *(u32x4*)(KN + (8 + pkr) * QS_STRIDE + pkc) = pk0;
        *(u32x4*)(KN + (24 + pkr) * QS_STRIDE + pkc) = pk1;
#pragma unroll
        for (int j = 0; j < 4; ++j) {
          VN[(pvc + 2 * j) * VT_STRIDE + 8 + pvr] = (u16)(pv[j] & 0xffff);
          VN[(pvc + 2 * j + 1) * VT_STRIDE + 8 + pvr] = (u16)(pv[j] >> 16);
        }
      }
    }
    if (fr < 8) {
      u16* kv = kvm + (size_t)(s * MEMR + fr) * 8192;
      if (half == 0) {
        *(u32x2*)(kv + h * DH + 32 * w + 4 * g) = o[2];
        *(u32x2*)(kv + h * DH + 32 * w + 16 + 4 * g) = o[3];
      }
      *(u32x2*)(kv + HD + h * DH + 64 * half + 16 * w + 4 * g) = o[4];
    }
  }
}

__global__ __launch_bounds__(256) void k_attn_x(const u16* __restrict__ qkvx, const u16* __restrict__ kvm,
                                                u16* __restrict__ ox) {
  __shared__ __attribute__((aligned(16))) u16 Vs[4][64][136];
  int w = threadIdx.x >> 6, l = threadIdx.x & 63, fr = l & 15, g = l >> 4;
  int s = blockIdx.x >> 3, h = (blockIdx.x & 7) * 4 + w;
  const u16* Qb = qkvx + (size_t)(s * SEG) * NQKV + h * DH;
  const u16* Kb = Qb + HD;
  const u16* Vb = Qb + 2 * HD;
  const u16* Km = kvm + (size_t)(s * MEMR) * 8192 + h * DH;
  const u16* Vm = Km + HD;
  for (int e = l; e < 64 * 16; e += 64) {
    int j = e >> 4, ch = (e & 15) * 8;
    u32x4 v = {0u, 0u, 0u, 0u};
    if (j < 8) v = *(const u32x4*)(Vm + (size_t)j * 8192 + ch);
    else if (j < 40) v = *(const u32x4*)(Vb + (size_t)(j - 8) * NQKV + ch);
    *(u32x4*)(&Vs[w][j][ch]) = v;
  }
  f32x4 sc[2][3] = {};
#pragma unroll
  for (int ks = 0; ks < 4; ++ks) {
    bf16x8 a[3], b[2];
#pragma unroll
    for (int t = 0; t < 3; ++t) {
      int j = 16 * t + fr;
      const u16* p = (j < 8) ? (Km + (size_t)j * 8192) : (Kb + (size_t)((j < 40 ? j : 39) - 8) * NQKV);
      a[t] = *(const bf16x8*)(p + 32 * ks + 8 * g);
    }
#pragma unroll
    for (int it = 0; it < 2; ++it) b[it] = *(const bf16x8*)(Qb + (size_t)(16 * it + fr) * NQKV + 32 * ks + 8 * g);
#pragma unroll
    for (int it = 0; it < 2; ++it)
#pragma unroll
      for (int t = 0; t < 3; ++t) sc[it][t] = MFMA16(a[t], b[it], sc[it][t]);
  }
  bf16x8 pb[2][2];
#pragma unroll
  for (int it = 0; it < 2; ++it) {
    int i = 16 * it + fr;
    float pr[3][4];
    float mx = -INFINITY;
#pragma unroll
    for (int t = 0; t < 3; ++t)
#pragma unroll
      for (int r = 0; r < 4; ++r) {
        int j = 16 * t + 4 * g + r;
        float v = rbf(rbf(sc[it][t][r]) * 0.08838834764831845f);
        if (j > 8 + i) v = -INFINITY;
        pr[t][r] = v;
        mx = fmaxf(mx, v);
      }
    mx = fmaxf(mx, __shfl_xor(mx, 16));
    mx = fmaxf(mx, __shfl_xor(mx, 32));
    float sum = 0.f;
#pragma unroll
    for (int t = 0; t < 3; ++t)
#pragma unroll
      for (int r = 0; r < 4; ++r) { float e = __expf(pr[t][r] - mx); pr[t][r] = e; sum += e; }
    sum += __shfl_xor(sum, 16);
    sum += __shfl_xor(sum, 32);
    float inv = 1.0f / sum;
#pragma unroll
    for (int r = 0; r < 4; ++r) {
      pb[it][0][r] = (short)f2bf(pr[0][r] * inv);
      pb[it][0][4 + r] = (short)f2bf(pr[1][r] * inv);
      pb[it][1][r] = (short)f2bf(pr[2][r] * inv);
      pb[it][1][4 + r] = 0;
    }
  }
  __syncthreads();
#pragma unroll
  for (int T = 0; T < 8; ++T) {
    f32x4 oa[2] = {};
#pragma unroll
    for (int u = 0; u < 2; ++u) {
      bf16x8 a;
#pragma unroll
      for (int i = 0; i < 8; ++i) a[i] = (short)Vs[w][32 * u + 16 * (i >> 2) + 4 * g + (i & 3)][16 * T + fr];
#pragma unroll
      for (int it = 0; it < 2; ++it) oa[it] = MFMA16(a, pb[it][u], oa[it]);
    }
#pragma unroll
    for (int it = 0; it < 2; ++it) {
      u32x2 o;
      o[0] = (u32)f2bf(oa[it][0]) | ((u32)f2bf(oa[it][1]) << 16);
      o[1] = (u32)f2bf(oa[it][2]) | ((u32)f2bf(oa[it][3]) << 16);
      *(u32x2*)(ox + (size_t)(s * SEG + 16 * it + fr) * HD + h * DH + 16 * T + 4 * g) = o;
    }
  }
}

extern "C" void kernel_launch(void* const* d_in, const int* in_sizes, int n_in, void* d_out, int out_size,
                              void* d_ws, size_t ws_size, hipStream_t stream) {
  const u16* x = (const u16*)d_in[0];
  const u16* mem0 = (const u16*)d_in[1];
  const u16* wq = (const u16*)d_in[2];
  const u16* wk = (const u16*)d_in[3];
  const u16* wv = (const u16*)d_in[4];
  const u16* wo = (const u16*)d_in[5];
  const u16* wr = (const u16*)d_in[8];
  const u16* wg = (const u16*)d_in[9];
  const u16* wu = (const u16*)d_in[10];
  const u16* wd = (const u16*)d_in[11];
  u16* out = (u16*)d_out;
  char* ws = (char*)d_ws;
  float* P = (float*)(ws + WS_P_OFF);
  u64* Mg = (u64*)(ws + WS_MG_OFF);
  u32* err = (u32*)(ws + WS_ERR_OFF);
  u16* Wt = (u16*)(ws + WS_WT);
  u16* Wot = (u16*)(ws + WS_WOT);
  u16* xn = (u16*)(ws + WS_XN);
  float* r = (float*)(ws + WS_R);
  u16* qkvx = (u16*)(ws + WS_QKV);
  u16* kvm = (u16*)(ws + WS_KVM);
  u16* ox = (u16*)(ws + WS_OX);
  int* idx = (int*)(ws + WS_IDX);
  float* hmid = (float*)(ws + WS_HMID);
  float* upd = (float*)(ws + WS_UPD);

  hipMemsetAsync(ws + WS_SYNC, 0, WS_SYNC_BYTES, stream);
  k_transpose<<<2560, 256, 0, stream>>>(wq, wk, wv, wo, Wt, Wot);
  k_ln<<<512, 256, 0, stream>>>(x, wr, xn, r);
  k_topk<<<1, 256, 0, stream>>>(r, idx);
  k_moe1<<<27, 1024, 0, stream>>>(xn, idx, wg, wu, hmid);
  k_moe2<<<10, 1024, 0, stream>>>(hmid, idx, r, wd, upd);
  k_gemm<0><<<16 * 96, 256, 0, stream>>>(xn, Wt, qkvx, NQKV, DM, nullptr, nullptr, nullptr, nullptr);
  hipFuncSetAttribute((const void*)k_recur, hipFuncAttributeMaxDynamicSharedMemorySize, LDS_TOTAL);
  k_recur<<<64, 256, LDS_TOTAL, stream>>>(Wt, Wot, mem0, qkvx, kvm, P, Mg, err);
  k_attn_x<<<512, 256, 0, stream>>>(qkvx, kvm, ox);
  k_gemm<1><<<16 * 5, 256, 0, stream>>>(ox, Wot, out, DM, HD, x, xn, idx, upd);
}
```

```cpp
#include <hip/hip_runtime.h>
#include <hip/hip_bf16.h>
#include <stdint.h>

#define S_TOK 2048
#define DM 640
#define NH 32
#define DH 128
#define HD 4096
#define NQKV 12288
#define MEMR 8
#define SEG 32
#define NSEG 64
#define FF 1728

typedef unsigned short u16;
typedef unsigned int u32;
typedef unsigned long long u64;
typedef __attribute__((ext_vector_type(8))) short bf16x8;
typedef __attribute__((ext_vector_type(4))) short bf16x4;
typedef __attribute__((ext_vector_type(4))) float f32x4;
typedef __attribute__((ext_vector_type(4))) unsigned int u32x4;
typedef __attribute__((ext_vector_type(2))) unsigned int u32x2;

__device__ __forceinline__ float bf2f(u16 v) { return __uint_as_float(((u32)v) << 16); }
__device__ __forceinline__ u16 f2bf(float f) { return __builtin_bit_cast(u16, (__bf16)f); }
__device__ __forceinline__ float rbf(float f) { return bf2f(f2bf(f)); }

#define ZERO4 (f32x4{0.f, 0.f, 0.f, 0.f})
#define MFMA16(a, b, c) __builtin_amdgcn_mfma_f32_16x16x32_bf16((a), (b), (c), 0, 0, 0)

#define WS_P_OFF 0
#define WS_MG_OFF (64 * 5120 * 4)
#define WS_FLAG_OFF (WS_MG_OFF + 2560 * 8)
#define FL_TILE 0
#define FL_STEP 1536
#define FL_ATTN (FL_STEP + 64 * 16)
#define FL_TOPK (FL_ATTN + 64 * 16)
#define FL_MOE1 (FL_TOPK + 16)
#define FL_MOE2 (FL_MOE1 + 16)
#define FL_ERR (FL_MOE2 + 16)
#define FL_WORDS (FL_ERR + 16)
#define WS_SYNC_BYTES (WS_FLAG_OFF + FL_WORDS * 4)
#define WS_ERR_OFF (WS_FLAG_OFF + FL_ERR * 4)
#define WS_WT 0x200000
#define WS_WOT (WS_WT + NQKV * DM * 2)
#define WS_XN (WS_WOT + DM * HD * 2)
#define WS_R (WS_XN + S_TOK * DM * 2)
#define WS_QKV (WS_R + S_TOK * 4)
#define WS_KVM (WS_QKV + (size_t)S_TOK * NQKV * 2)
#define WS_OX (WS_KVM + (size_t)NSEG * MEMR * 8192 * 2)
#define WS_IDX (WS_OX + (size_t)S_TOK * HD * 2)
#define WS_HMID (WS_IDX + 256)
#define WS_UPD (WS_HMID + 8 * FF * 4)

#define N_REC 64
#define N_RED 80
#define N_WRK 112

__global__ __launch_bounds__(256) void k_prep(const u16* __restrict__ wq, const u16* __restrict__ wk,
                                              const u16* __restrict__ wv, const u16* __restrict__ wo,
                                              const u16* __restrict__ x, const u16* __restrict__ wr, char* ws) {
  __shared__ u16 tile[64][66];
  int b = blockIdx.x;
  int t = threadIdx.x;
  if (b < 2560) {
    u16* wt = (u16*)(ws + WS_WT);
    u16* wot = (u16*)(ws + WS_WOT);
    const u16* src;
    u16* dst;
    int C, R, tr, tc;
    if (b < 1920) {
      int which = b / 640, rem = b % 640;
      src = which == 0 ? wq : (which == 1 ? wk : wv);
      dst = wt + (size_t)which * HD * DM;
      R = DM; C = HD;
      tr = rem / 64; tc = rem % 64;
    } else {
      int rem = b - 1920;
      src = wo; dst = wot;
      R = HD; C = DM;
      tr = rem / 10; tc = rem % 10;
    }
#pragma unroll
    for (int i = 0; i < 2; ++i) {
      int r = (t >> 3) + 32 * i, c8 = (t & 7) * 8;
      u32x4 v = *(const u32x4*)(src + (size_t)(tr * 64 + r) * C + tc * 64 + c8);
      u16* p = &tile[r][c8];
#pragma unroll
      for (int j = 0; j < 4; ++j) { p[2 * j] = (u16)(v[j] & 0xffff); p[2 * j + 1] = (u16)(v[j] >> 16); }
    }
    __syncthreads();
#pragma unroll
    for (int i = 0; i < 2; ++i) {
      int c = (t >> 3) + 32 * i, r8 = (t & 7) * 8;
      u32x4 v;
#pragma unroll
      for (int j = 0; j < 4; ++j) v[j] = (u32)tile[r8 + 2 * j][c] | ((u32)tile[r8 + 2 * j + 1][c] << 16);
      *(u32x4*)(dst + (size_t)(tc * 64 + c) * R + tr * 64 + r8) = v;
    }
  } else if (b < 3072) {
    u16* xn = (u16*)(ws + WS_XN);
    float* r = (float*)(ws + WS_R);
    int row = (b - 2560) * 4 + (t >> 6), l = t & 63;
    const u32* xr = (const u32*)(x + (size_t)row * DM);
    float v[10];
    float sum = 0.f;
#pragma unroll
    for (int i = 0; i < 5; ++i) {
      u32 u = xr[l + 64 * i];
      v[2 * i] = bf2f((u16)(u & 0xffff));
      v[2 * i + 1] = bf2f((u16)(u >> 16));
      sum += v[2 * i] + v[2 * i + 1];
    }
#pragma unroll
    for (int o = 32; o > 0; o >>= 1) sum += __shfl_xor(sum, o);
    float mean = sum * (1.0f / DM);
    float sq = 0.f;
#pragma unroll
    for (int i = 0; i < 10; ++i) { float d = v[i] - mean; sq += d * d; }
#pragma unroll
    for (int o = 32; o > 0; o >>= 1) sq += __shfl_xor(sq, o);
    float var = sq * (1.0f / DM);
    float mb = rbf(mean), vb = rbf(var);
    float den = rbf(sqrtf(rbf(vb + 1e-5f)));
    float acc = 0.f;
    u32* xo = (u32*)(xn + (size_t)row * DM);
    const u32* wr2 = (const u32*)wr;
#pragma unroll
    for (int i = 0; i < 5; ++i) {
      u16 a = f2bf(rbf(v[2 * i] - mb) / den);
      u16 bb = f2bf(rbf(v[2 * i + 1] - mb) / den);
      xo[l + 64 * i] = (u32)a | ((u32)bb << 16);
      u32 w = wr2[l + 64 * i];
      acc += bf2f(a) * bf2f((u16)(w & 0xffff)) + bf2f(bb) * bf2f((u16)(w >> 16));
    }
#pragma unroll
    for (int o = 32; o > 0; o >>= 1) acc += __shfl_xor(acc, o);
    if (l == 0) r[row] = rbf(acc);
  } else {
    u32x4 z = {0u, 0u, 0u, 0u};
    int base = (b - 3072) * 512 + t;
#pragma unroll
    for (int i = 0; i < 2; ++i) {
      int e = base + 256 * i;
      if (e < WS_SYNC_BYTES / 16) ((u32x4*)ws)[e] = z;
    }
  }
}

#define MS_STRIDE 672
#define QS_STRIDE 136
#define VT_STRIDE 72
#define KS_ELEMS (48 * QS_STRIDE)
#define VT_ELEMS (64 * VT_STRIDE)
#define LDS_VW 0
#define LDS_MEM (LDS_VW + 81920)
#define LDS_Q (LDS_MEM + 10752)
#define LDS_K (LDS_Q + 2176)
#define LDS_VT (LDS_K + 26112)
#define LDS_WO (LDS_VT + 18432)
#define LDS_TOTAL (LDS_WO + 16384)

#define SPIN_CAP (1u << 17)

#define MFMA_A0(acc, wa, b) asm("v_mfma_f32_16x16x32_bf16 %0, %1, %2, 0" : "=&v"(acc) : "a"(wa), "v"(b))
#define MFMA_A(acc, wa, b) asm("v_mfma_f32_16x16x32_bf16 %0, %1, %2, %0" : "+v"(acc) : "a"(wa), "v"(b))
#define MFMA_V0(acc, wa, b) asm("v_mfma_f32_16x16x32_bf16 %0, %1, %2, 0" : "=&v"(acc) : "v"(wa), "v"(b))
#define MFMA_V(acc, wa, b) asm("v_mfma_f32_16x16x32_bf16 %0, %1, %2, %0" : "+v"(acc) : "v"(wa), "v"(b))

__device__ __forceinline__ float red_max_rows(float x) {
  u32 u = __float_as_uint(x);
  auto a = __builtin_amdgcn_permlane16_swap(u, u, false, false);
  float m = fmaxf(__uint_as_float(a[0]), __uint_as_float(a[1]));
  u = __float_as_uint(m);
  auto b = __builtin_amdgcn_permlane32_swap(u, u, false, false);
  return fmaxf(__uint_as_float(b[0]), __uint_as_float(b[1]));
}
__device__ __forceinline__ float red_sum_rows(float x) {
  u32 u = __float_as_uint(x);
  auto a = __builtin_amdgcn_permlane16_swap(u, u, false, false);
  float m = __uint_as_float(a[0]) + __uint_as_float(a[1]);
  u = __float_as_uint(m);
  auto b = __builtin_amdgcn_permlane32_swap(u, u, false, false);
  return __uint_as_float(b[0]) + __uint_as_float(b[1]);
}
__device__ __forceinline__ u32 pack2(float a, float b) {
  u32 r;
  asm("v_cvt_pk_bf16_f32 %0, %1, %2" : "=v"(r) : "v"(a), "v"(b));
  return r;
}
__device__ __forceinline__ u32 ld_agent(const u32* p) { return __hip_atomic_load(p, __ATOMIC_RELAXED, __HIP_MEMORY_SCOPE_AGENT); }
__device__ __forceinline__ void st_agent(u32* p, u32 v) { __hip_atomic_store(p, v, __ATOMIC_RELAXED, __HIP_MEMORY_SCOPE_AGENT); }
__device__ __forceinline__ void add_agent(u32* p, u32 v) { __hip_atomic_fetch_add(p, v, __ATOMIC_RELAXED, __HIP_MEMORY_SCOPE_AGENT); }
#define DRAIN() asm volatile("s_waitcnt vmcnt(0)" ::: "memory")

__device__ __forceinline__ void role_recur(char* smem, char* ws, const u16* __restrict__ mem0, int c) {
  u16* vW = (u16*)(smem + LDS_VW);
  u16* memS = (u16*)(smem + LDS_MEM);
  u16* qS = (u16*)(smem + LDS_Q);
  u16* KSb = (u16*)(smem + LDS_K);
  u16* VTb = (u16*)(smem + LDS_VT);
  u16* woL = (u16*)(smem + LDS_WO);
  const u16* Wt = (const u16*)(ws + WS_WT);
  const u16* Wot = (const u16*)(ws + WS_WOT);
  u32* flags = (u32*)(ws + WS_FLAG_OFF);
  const int h = c >> 1, half = c & 1;

  __amdgpu_buffer_rsrc_t rP = __builtin_amdgcn_make_buffer_rsrc((void*)(ws + WS_P_OFF), 0, 64 * 5120 * 4, 0x00020000);
  __amdgpu_buffer_rsrc_t rM = __builtin_amdgcn_make_buffer_rsrc((void*)(ws + WS_MG_OFF), 0, 2560 * 8, 0x00020000);
  __amdgpu_buffer_rsrc_t rQ = __builtin_amdgcn_make_buffer_rsrc((void*)(ws + WS_QKV), 0, S_TOK * NQKV * 2, 0x00020000);
  __amdgpu_buffer_rsrc_t rK = __builtin_amdgcn_make_buffer_rsrc((void*)(ws + WS_KVM), 0, NSEG * MEMR * 8192 * 2, 0x00020000);

  bool dead = false;
  bf16x8 wa[3][20];
  bf16x8 wv3[20];
  bf16x8 woa[2][2];
  bf16x8 wov[6][2];
  u32x4 pk0, pk1, pv;
  {
    const int tid = threadIdx.x, w = tid >> 6, l = tid & 63, fr = l & 15, g = l >> 4;
#pragma unroll
    for (int t = 0; t < 4; ++t) {
      const u16* wp = Wt + (size_t)((t >> 1) * HD + h * DH + 32 * w + 16 * (t & 1) + fr) * DM + 8 * g;
#pragma unroll
      for (int ks = 0; ks < 20; ++ks) {
        bf16x8 f = *(const bf16x8*)(wp + 32 * ks);
        if (t < 3) wa[t < 3 ? t : 0][ks] = f; else wv3[ks] = f;
      }
    }
    {
      const u16* wp = Wt + (size_t)(2 * HD + h * DH + 64 * half + 16 * w + fr) * DM + 8 * g;
#pragma unroll
      for (int ks = 0; ks < 20; ++ks) *(bf16x8*)(vW + (w * 20 + ks) * 512 + l * 8) = *(const bf16x8*)(wp + 32 * ks);
    }
#pragma unroll
    for (int T = 0; T < 10; ++T) {
      const u16* wp = Wot + (size_t)(160 * w + 16 * T + fr) * HD + h * DH + 64 * half + 4 * g;
#pragma unroll
      for (int u = 0; u < 2; ++u) {
        bf16x4 lo = *(const bf16x4*)(wp + 32 * u);
        bf16x4 hi = *(const bf16x4*)(wp + 32 * u + 16);
        bf16x8 f = __builtin_shufflevector(lo, hi, 0, 1, 2, 3, 4, 5, 6, 7);
        if (T < 2) woa[T < 2 ? T : 0][u] = f;
        else if (T < 8) wov[(T >= 2 && T < 8) ? T - 2 : 0][u] = f;
        else *(bf16x8*)(woL + (w * 4 + (T - 8) * 2 + u) * 512 + l * 8) = f;
      }
    }
    for (int e = tid; e < 8 * QS_STRIDE; e += 256) { KSb[40 * QS_STRIDE + e] = 0; KSb[KS_ELEMS + 40 * QS_STRIDE + e] = 0; }
    for (int e = tid; e < 64 * 32; e += 256) {
      VTb[(e >> 5) * VT_STRIDE + 40 + (e & 31)] = 0;
      VTb[VT_ELEMS + (e >> 5) * VT_STRIDE + 40 + (e & 31)] = 0;
    }
    {
      u32 spins = 0;
      while ((ld_agent(flags + FL_TILE + 32 + h) & ld_agent(flags + FL_TILE + 64 + h)) == 0) {
        __builtin_amdgcn_s_sleep(2);
        if (++spins > SPIN_CAP) { if (l == 0) atomicOr(flags + FL_ERR, 4u); dead = true; break; }
      }
    }
    const int pkr = tid >> 4, pkc = (tid & 15) * 8, pvr = tid >> 3, pvc = (tid & 7) * 8;
    const int ko = (pkr * NQKV + HD + h * DH + pkc) * 2;
    pk0 = __builtin_amdgcn_raw_buffer_load_b128(rQ, ko, 0, 16);
    pk1 = __builtin_amdgcn_raw_buffer_load_b128(rQ, ko + 16 * NQKV * 2, 0, 16);
    pv = __builtin_amdgcn_raw_buffer_load_b128(rQ, (pvr * NQKV + 2 * HD + h * DH + 64 * half + pvc) * 2, 0, 16);
    *(u32x4*)(KSb + (8 + pkr) * QS_STRIDE + pkc) = pk0;
    *(u32x4*)(KSb + (24 + pkr) * QS_STRIDE + pkc) = pk1;
#pragma unroll
    for (int j = 0; j < 4; ++j) {
      VTb[(pvc + 2 * j) * VT_STRIDE + 8 + pvr] = (u16)(pv[j] & 0xffff);
      VTb[(pvc + 2 * j + 1) * VT_STRIDE + 8 + pvr] = (u16)(pv[j] >> 16);
    }
  }

  for (int s = 0; s < NSEG; ++s) {
    int tz = threadIdx.x;
    asm volatile("" : "+v"(tz));
    const int tid = tz, w = tz >> 6, l = tz & 63, fr = l & 15, g = l >> 4;
    const int pkr = tid >> 4, pkc = (tid & 15) * 8;
    const int pvr = tid >> 3, pvc = (tid & 7) * 8;
    u16* KS = KSb + (s & 1) * KS_ELEMS;
    u16* VT = VTb + (s & 1) * VT_ELEMS;
    if (s == 0) {
#pragma unroll
      for (int i = 0; i < 5; ++i) {
        int f = 4 * (tid + 256 * i);
        int m = f / DM, col = f % DM;
        *(u32x2*)(memS + m * MS_STRIDE + col) = *(const u32x2*)(mem0 + f);
      }
    } else {
      u32x4 v[5];
      u32 spins = 0;
      for (;;) {
        u32 bad = 0;
#pragma unroll
        for (int i = 0; i < 5; ++i) v[i] = __builtin_amdgcn_raw_buffer_load_b128(rM, (tid + 256 * i) * 16, 0, 16);
#pragma unroll
        for (int i = 0; i < 5; ++i) bad |= (v[i][1] ^ (u32)s) | (v[i][3] ^ (u32)s);
        if (__all(bad == 0)) break;
        asm volatile("" ::: "memory");
        if (dead || ++spins > SPIN_CAP) { if (l == 0) atomicOr(flags + FL_ERR, 1u); dead = true; break; }
      }
#pragma unroll
      for (int i = 0; i < 5; ++i) {
        int f = 4 * (tid + 256 * i);
        int m = f / DM, col = f % DM;
        u32x2 o; o[0] = v[i][0]; o[1] = v[i][2];
        *(u32x2*)(memS + m * MS_STRIDE + col) = o;
      }
      DRAIN();
    }
    __syncthreads();
    if (s > 0 && tid == 0) add_agent(flags + FL_STEP + (s - 1) * 16, 1u);

    if (s + 1 < NSEG) {
      const int ko = (((s + 1) * SEG + pkr) * NQKV + HD + h * DH + pkc) * 2;
      pk0 = __builtin_amdgcn_raw_buffer_load_b128(rQ, ko, 0, 16);
      pk1 = __builtin_amdgcn_raw_buffer_load_b128(rQ, ko + 16 * NQKV * 2, 0, 16);
      pv = __builtin_amdgcn_raw_buffer_load_b128(rQ, (((s + 1) * SEG + pvr) * NQKV + 2 * HD + h * DH + 64 * half + pvc) * 2, 0, 16);
    }

    f32x4 acc[5];
    {
      bf16x8 b[3][2], vf[3][2];
      const u16* mp = memS + (fr & 7) * MS_STRIDE + 8 * g;
      const u16* vp = vW + (w * 20) * 512 + l * 8;
#pragma unroll
      for (int i = 0; i < 4; ++i) { b[i >> 1][i & 1] = *(const bf16x8*)(mp + 32 * i); vf[i >> 1][i & 1] = *(const bf16x8*)(vp + 512 * i); }
#pragma unroll
      for (int ch = 0; ch < 10; ++ch) {
        if (ch < 8) {
#pragma unroll
          for (int i = 0; i < 2; ++i) {
            b[(ch + 2) % 3][i] = *(const bf16x8*)(mp + 32 * (2 * ch + 4 + i));
            vf[(ch + 2) % 3][i] = *(const bf16x8*)(vp + 512 * (2 * ch + 4 + i));
          }
        }
        __builtin_amdgcn_sched_barrier(0);
#pragma unroll
        for (int i = 0; i < 2; ++i) {
          const int ks = 2 * ch + i;
          if (ks == 0) {
            MFMA_A0(acc[0], wa[0][0], b[0][0]);
            MFMA_A0(acc[1], wa[1][0], b[0][0]);
            MFMA_A0(acc[2], wa[2][0], b[0][0]);
            MFMA_V0(acc[3], wv3[0], b[0][0]);
            MFMA_V0(acc[4], vf[0][0], b[0][0]);
          } else {
            MFMA_A(acc[0], wa[0][ks], b[ch % 3][i]);
            MFMA_A(acc[1], wa[1][ks], b[ch % 3][i]);
            MFMA_A(acc[2], wa[2][ks], b[ch % 3][i]);
            MFMA_V(acc[3], wv3[ks], b[ch % 3][i]);
            MFMA_V(acc[4], vf[ch % 3][i], b[ch % 3][i]);
          }
        }
        __builtin_amdgcn_sched_barrier(0);
      }
      asm volatile("s_nop 7\n\ts_nop 7" : "+v"(acc[0]), "+v"(acc[1]), "+v"(acc[2]), "+v"(acc[3]), "+v"(acc[4]));
    }
    u32x2 o[5];
#pragma unroll
    for (int t = 0; t < 5; ++t) { o[t][0] = pack2(acc[t][0], acc[t][1]); o[t][1] = pack2(acc[t][2], acc[t][3]); }
    if (fr < 8) {
      *(u32x2*)(qS + fr * QS_STRIDE + 32 * w + 4 * g) = o[0];
      *(u32x2*)(qS + fr * QS_STRIDE + 32 * w + 16 + 4 * g) = o[1];
      *(u32x2*)(KS + fr * QS_STRIDE + 32 * w + 4 * g) = o[2];
      *(u32x2*)(KS + fr * QS_STRIDE + 32 * w + 16 + 4 * g) = o[3];
      VT[(16 * w + 4 * g + 0) * VT_STRIDE + fr] = (u16)(o[4][0] & 0xffff);
      VT[(16 * w + 4 * g + 1) * VT_STRIDE + fr] = (u16)(o[4][0] >> 16);
      VT[(16 * w + 4 * g + 2) * VT_STRIDE + fr] = (u16)(o[4][1] & 0xffff);
      VT[(16 * w + 4 * g + 3) * VT_STRIDE + fr] = (u16)(o[4][1] >> 16);
    }
    if (s + 1 < NSEG) {
      __syncthreads();

      f32x4 sc[3];
      bf16x8 va[4][2];
      {
        bf16x8 qb[4], ka[3][4];
#pragma unroll
        for (int ks = 0; ks < 4; ++ks) {
          qb[ks] = *(const bf16x8*)(qS + (fr & 7) * QS_STRIDE + 32 * ks + 8 * g);
#pragma unroll
          for (int t = 0; t < 3; ++t) ka[t][ks] = *(const bf16x8*)(KS + (16 * t + fr) * QS_STRIDE + 32 * ks + 8 * g);
        }
#pragma unroll
        for (int t = 0; t < 3; ++t) MFMA_V0(sc[t], ka[t][0], qb[0]);
#pragma unroll
        for (int ks = 1; ks < 4; ++ks)
#pragma unroll
          for (int t = 0; t < 3; ++t) MFMA_V(sc[t], ka[t][ks], qb[ks]);
        __builtin_amdgcn_sched_barrier(0);
#pragma unroll
        for (int T = 0; T < 4; ++T)
#pragma unroll
          for (int u = 0; u < 2; ++u) {
            bf16x4 lo = *(const bf16x4*)(VT + (16 * T + fr) * VT_STRIDE + 32 * u + 4 * g);
            bf16x4 hi = *(const bf16x4*)(VT + (16 * T + fr) * VT_STRIDE + 32 * u + 16 + 4 * g);
            va[T][u] = __builtin_shufflevector(lo, hi, 0, 1, 2, 3, 4, 5, 6, 7);
          }
        asm volatile("s_nop 7\n\ts_nop 7" : "+v"(sc[0]), "+v"(sc[1]), "+v"(sc[2]));
      }
      float pr[12];
#pragma unroll
      for (int t = 0; t < 3; ++t)
#pragma unroll
        for (int r2 = 0; r2 < 2; ++r2) {
          u32 p1 = pack2(sc[t][2 * r2], sc[t][2 * r2 + 1]);
          float a0 = __uint_as_float(p1 << 16) * 0.08838834764831845f;
          float a1 = __uint_as_float(p1 & 0xffff0000u) * 0.08838834764831845f;
          u32 p2 = pack2(a0, a1);
          pr[4 * t + 2 * r2] = __uint_as_float(p2 << 16);
          pr[4 * t + 2 * r2 + 1] = __uint_as_float(p2 & 0xffff0000u);
        }
      if (g >= 2) { pr[8] = -INFINITY; pr[9] = -INFINITY; pr[10] = -INFINITY; pr[11] = -INFINITY; }
      float mx = fmaxf(fmaxf(fmaxf(pr[0], pr[1]), fmaxf(pr[2], pr[3])), fmaxf(fmaxf(pr[4], pr[5]), fmaxf(pr[6], pr[7])));
      mx = fmaxf(mx, fmaxf(fmaxf(pr[8], pr[9]), fmaxf(pr[10], pr[11])));
      mx = red_max_rows(mx);
      const float nmx = -mx * 1.4426950408889634f;
#pragma unroll
      for (int i = 0; i < 12; ++i) { pr[i] = __builtin_amdgcn_exp2f(fmaf(pr[i], 1.4426950408889634f, nmx)); }
      float sum = ((pr[0] + pr[1]) + (pr[2] + pr[3])) + ((pr[4] + pr[5]) + (pr[6] + pr[7])) + ((pr[8] + pr[9]) + (pr[10] + pr[11]));
      sum = red_sum_rows(sum);
      float inv = __builtin_amdgcn_rcpf(sum);
      bf16x8 pb[2];
      {
        u32x4 t0, t1;
        t0[0] = pack2(pr[0] * inv, pr[1] * inv); t0[1] = pack2(pr[2] * inv, pr[3] * inv);
        t0[2] = pack2(pr[4] * inv, pr[5] * inv); t0[3] = pack2(pr[6] * inv, pr[7] * inv);
        t1[0] = pack2(pr[8] * inv, pr[9] * inv); t1[1] = pack2(pr[10] * inv, pr[11] * inv);
        t1[2] = 0; t1[3] = 0;
        pb[0] = __builtin_bit_cast(bf16x8, t0); pb[1] = __builtin_bit_cast(bf16x8, t1);
      }
      bf16x8 ob[2];
      bf16x8 wl[4];
      {
        f32x4 oa[4];
        asm volatile("s_nop 1" : "+v"(pb[0]), "+v"(pb[1]));
#pragma unroll
        for (int T = 0; T < 4; ++T) MFMA_V0(oa[T], va[T][0], pb[0]);
#pragma unroll
        for (int T = 0; T < 4; ++T) MFMA_V(oa[T], va[T][1], pb[1]);
        __builtin_amdgcn_sched_barrier(0);
#pragma unroll
        for (int i = 0; i < 4; ++i) wl[i] = *(const bf16x8*)(woL + (w * 4 + i) * 512 + l * 8);
        asm volatile("s_nop 7\n\ts_nop 7" : "+v"(oa[0]), "+v"(oa[1]), "+v"(oa[2]), "+v"(oa[3]));
        u32x4 t0, t1;
        t0[0] = pack2(oa[0][0], oa[0][1]); t0[1] = pack2(oa[0][2], oa[0][3]);
        t0[2] = pack2(oa[1][0], oa[1][1]); t0[3] = pack2(oa[1][2], oa[1][3]);
        t1[0] = pack2(oa[2][0], oa[2][1]); t1[1] = pack2(oa[2][2], oa[2][3]);
        t1[2] = pack2(oa[3][0], oa[3][1]); t1[3] = pack2(oa[3][2], oa[3][3]);
        ob[0] = __builtin_bit_cast(bf16x8, t0); ob[1] = __builtin_bit_cast(bf16x8, t1);
      }
      const u32 tagp = (u32)(s % 3) + 1u;
      {
        f32x4 pa[10];
        asm volatile("s_nop 1" : "+v"(ob[0]), "+v"(ob[1]));
        MFMA_A0(pa[0], woa[0][0], ob[0]);
        MFMA_A0(pa[1], woa[1][0], ob[0]);
#pragma unroll
        for (int T = 2; T < 8; ++T) MFMA_V0(pa[T], wov[T - 2][0], ob[0]);
        MFMA_V0(pa[8], wl[0], ob[0]);
        MFMA_V0(pa[9], wl[2], ob[0]);
        MFMA_A(pa[0], woa[0][1], ob[1]);
        MFMA_A(pa[1], woa[1][1], ob[1]);
#pragma unroll
        for (int T = 2; T < 8; ++T) MFMA_V(pa[T], wov[T - 2][1], ob[1]);
        MFMA_V(pa[8], wl[1], ob[1]);
        MFMA_V(pa[9], wl[3], ob[1]);
        asm volatile("s_nop 7\n\ts_nop 7" : "+v"(pa[0]), "+v"(pa[1]), "+v"(pa[2]), "+v"(pa[3]), "+v"(pa[4]),
                     "+v"(pa[5]), "+v"(pa[6]), "+v"(pa[7]), "+v"(pa[8]), "+v"(pa[9]));
        const int soff = (c * 5120 + (fr & 7) * DM + 160 * w + 16 * (fr >> 3) + 4 * g) * 4;
#pragma unroll
        for (int T = 0; T < 10; T += 2) {
          u32x4 ov;
#pragma unroll
          for (int r = 0; r < 4; ++r) {
            u32 lo = __float_as_uint(pa[T][r]);
            u32 hi = __builtin_amdgcn_update_dpp(0u, __float_as_uint(pa[T + 1][r]), 0x118, 0xf, 0xf, false);
            u32 v = (fr < 8) ? lo : hi;
            ov[r] = (v & ~3u) | tagp;
          }
          __builtin_amdgcn_raw_buffer_store_b128(ov, rP, soff + 64 * T, 0, 16);
        }
      }
      {
        u16* KN = KSb + ((s + 1) & 1) * KS_ELEMS;
        u16* VN = VTb + ((s + 1) & 1) * VT_ELEMS;
        *(u32x4*)(KN + (8 + pkr) * QS_STRIDE + pkc) = pk0;
        *(u32x4*)(KN + (24 + pkr) * QS_STRIDE + pkc) = pk1;
#pragma unroll
        for (int j = 0; j < 4; ++j) {
          VN[(pvc + 2 * j) * VT_STRIDE + 8 + pvr] = (u16)(pv[j] & 0xffff);
          VN[(pvc + 2 * j + 1) * VT_STRIDE + 8 + pvr] = (u16)(pv[j] >> 16);
        }
      }
    }
    if (fr < 8) {
      const int kvo = ((s * MEMR + fr) * 8192 + h * DH) * 2;
      if (half == 0) {
        __builtin_amdgcn_raw_buffer_store_b64(o[2], rK, kvo + (32 * w + 4 * g) * 2, 0, 16);
        __builtin_amdgcn_raw_buffer_store_b64(o[3], rK, kvo + (32 * w + 16 + 4 * g) * 2, 0, 16);
      }
      __builtin_amdgcn_raw_buffer_store_b64(o[4], rK, kvo + (HD + 64 * half + 16 * w + 4 * g) * 2, 0, 16);
    }
    if (((s + 2) & 3) == 0 && s + 2 < NSEG) {
      const int rb = (s + 2) >> 2;
      u32 spins = 0;
      while ((ld_agent(flags + FL_TILE + rb * 96 + 32 + h) & ld_agent(flags + FL_TILE + rb * 96 + 64 + h)) == 0) {
        __builtin_amdgcn_s_sleep(1);
        if (dead || ++spins > SPIN_CAP) { if (l == 0) atomicOr(flags + FL_ERR, 8u); dead = true; break; }
      }
    }
  }
  DRAIN();
  __syncthreads();
  if (threadIdx.x == 0) add_agent(flags + FL_STEP + (NSEG - 1) * 16, 1u);
}

__device__ __forceinline__ void role_reduce(char* smem, char* ws, int r) {
  float* red = (float*)smem;
  u32* flags = (u32*)(ws + WS_FLAG_OFF);
  __amdgpu_buffer_rsrc_t rP = __builtin_amdgcn_make_buffer_rsrc((void*)(ws + WS_P_OFF), 0, 64 * 5120 * 4, 0x00020000);
  __amdgpu_buffer_rsrc_t rM = __builtin_amdgcn_make_buffer_rsrc((void*)(ws + WS_MG_OFF), 0, 2560 * 8, 0x00020000);
  const int tid = threadIdx.x, w = tid >> 6, l = tid & 63;
  const int base = ((16 * w + (l >> 4)) * 5120 + r * 64 + (l & 15) * 4) * 4;
  bool dead = false;
  for (int s = 0; s + 1 < NSEG; ++s) {
    const u32 tagp = (u32)(s % 3) + 1u;
    u32x4 v[4];
    u32 spins = 0;
    for (;;) {
      u32 bad = 0;
#pragma unroll
      for (int i = 0; i < 4; ++i) v[i] = __builtin_amdgcn_raw_buffer_load_b128(rP, base + i * 4 * 5120 * 4, 0, 16);
#pragma unroll
      for (int i = 0; i < 4; ++i) bad |= ((v[i][0] ^ tagp) | (v[i][1] ^ tagp) | (v[i][2] ^ tagp) | (v[i][3] ^ tagp)) & 3u;
      if (__all(bad == 0)) break;
      asm volatile("" ::: "memory");
      if (dead || ++spins > 8 * SPIN_CAP) { if (l == 0) atomicOr(flags + FL_ERR, 2u); dead = true; break; }
    }
    f32x4 a;
#pragma unroll
    for (int q = 0; q < 4; ++q)
      a[q] = red_sum_rows((__uint_as_float(v[0][q]) + __uint_as_float(v[1][q])) + (__uint_as_float(v[2][q]) + __uint_as_float(v[3][q])));
    float* rd = red + (s & 1) * 256;
    if (l < 16) *(f32x4*)(rd + w * 64 + l * 4) = a;
    __syncthreads();
    if (w == 0 && l < 16) {
      f32x4 x = (*(f32x4*)(rd + l * 4) + *(f32x4*)(rd + 64 + l * 4)) + (*(f32x4*)(rd + 128 + l * 4) + *(f32x4*)(rd + 192 + l * 4));
      u32x4 og;
      og[0] = pack2(x[0], x[1]); og[1] = (u32)(s + 1);
      og[2] = pack2(x[2], x[3]); og[3] = (u32)(s + 1);
      __builtin_amdgcn_raw_buffer_store_b128(og, rM, (r * 32 + l * 2) * 8, 0, 16);
    }
  }
}

__device__ __forceinline__ void wait_word_ge(u32* flags, int word, u32 val, bool& dead) {
  u32 spins = 0;
  while (ld_agent(flags + word) < val) {
    __builtin_amdgcn_s_sleep(8);
    if (dead || ++spins > 16 * SPIN_CAP) { atomicOr(flags + FL_ERR, 16u); dead = true; break; }
  }
}
__device__ __forceinline__ void acquire_all() {
  __builtin_amdgcn_fence(__ATOMIC_ACQUIRE, "agent");
  DRAIN();
  __syncthreads();
}

__device__ __forceinline__ void task_qkv_tile(char* smem, char* ws, int rb, int cb) {
  const u16* A = (const u16*)(ws + WS_XN);
  const u16* Bt = (const u16*)(ws + WS_WT);
  u32* flags = (u32*)(ws + WS_FLAG_OFF);
  __amdgpu_buffer_rsrc_t rQ = __builtin_amdgcn_make_buffer_rsrc((void*)(ws + WS_QKV), 0, S_TOK * NQKV * 2, 0x00020000);
  const int K = DM;
  int brow = rb * 128, bcol = cb * 128;
  int wid = threadIdx.x >> 6, lane = threadIdx.x & 63, wr = wid >> 1, wc = wid & 1, fr = lane & 15, fq = lane >> 4;
  f32x4 acc[4][4] = {};
  auto stage = [&](int t, int buf) {
    char* SA = smem + buf * 16384;
    char* SB = SA + 8192;
#pragma unroll
    for (int i = 0; i < 2; ++i) {
      int b = threadIdx.x * 16 + i * 4096, r = b / 64, c = (b % 64) / 2;
      __builtin_amdgcn_global_load_lds((const u32*)(A + (size_t)(brow + r) * K + t * 32 + c), (u32*)(SA + b), 16, 0, 0);
      __builtin_amdgcn_global_load_lds((const u32*)(Bt + (size_t)(bcol + r) * K + t * 32 + c), (u32*)(SB + b), 16, 0, 0);
    }
  };
  stage(0, 0);
  for (int t = 0; t < K / 32; ++t) {
    DRAIN();
    __syncthreads();
    if (t + 1 < K / 32) stage(t + 1, (t + 1) & 1);
    char* SA = smem + (t & 1) * 16384;
    char* SB = SA + 8192;
    bf16x8 At[4], Bf[4];
#pragma unroll
    for (int m = 0; m < 4; ++m) {
      At[m] = *(const bf16x8*)(SA + (wr * 64 + m * 16 + fr) * 64 + fq * 16);
      Bf[m] = *(const bf16x8*)(SB + (wc * 64 + m * 16 + fr) * 64 + fq * 16);
    }
#pragma unroll
    for (int m = 0; m < 4; ++m)
#pragma unroll
      for (int n = 0; n < 4; ++n) acc[m][n] = MFMA16(Bf[n], At[m], acc[m][n]);
  }
#pragma unroll
  for (int m = 0; m < 4; ++m) {
    int row = brow + wr * 64 + m * 16 + fr;
#pragma unroll
    for (int n = 0; n < 4; ++n) {
      int col = bcol + wc * 64 + n * 16 + fq * 4;
      u32x2 o;
      o[0] = pack2(acc[m][n][0], acc[m][n][1]);
      o[1] = pack2(acc[m][n][2], acc[m][n][3]);
      __builtin_amdgcn_raw_buffer_store_b64(o, rQ, (row * NQKV + col) * 2, 0, 16);
    }
  }
  DRAIN();
  __syncthreads();
  if (threadIdx.x == 0) st_agent(flags + FL_TILE + rb * 96 + cb, 1u);
}

__device__ __forceinline__ void task_topk(char* ws) {
  const float* r = (const float*)(ws + WS_R);
  u32* idx = (u32*)(ws + WS_IDX);
  u32* flags = (u32*)(ws + WS_FLAG_OFF);
  if (threadIdx.x < 64) {
    int l = threadIdx.x;
    float v[32];
#pragma unroll
    for (int i = 0; i < 32; ++i) v[i] = r[l + 64 * i];
    u32 mine = 0;
    for (int k = 0; k < 8; ++k) {
      float bv = v[0]; int bi = l;
#pragma unroll
      for (int i = 1; i < 32; ++i) if (v[i] > bv) { bv = v[i]; bi = l + 64 * i; }
#pragma unroll
      for (int o = 32; o > 0; o >>= 1) {
        float ov = __shfl_xor(bv, o); int oi = __shfl_xor(bi, o);
        if (ov > bv || (ov == bv && oi < bi)) { bv = ov; bi = oi; }
      }
      if (l == k) mine = (u32)bi;
#pragma unroll
      for (int i = 0; i < 32; ++i) if (l + 64 * i == bi) v[i] = -INFINITY;
    }
    if (l < 8) st_agent(idx + l, mine);
    DRAIN();
    if (l == 0) st_agent(flags + FL_TOPK, 1u);
  }
}

__device__ __forceinline__ void task_moe1(char* smem, char* ws, const u16* __restrict__ wg, const u16* __restrict__ wu,
                                          int cbk, bool& dead) {
  const u16* xn = (const u16*)(ws + WS_XN);
  u32* idx = (u32*)(ws + WS_IDX);
  u32* hmid = (u32*)(ws + WS_HMID);
  u32* flags = (u32*)(ws + WS_FLAG_OFF);
  float* xs = (float*)smem;
  float* red = xs + 8 * DM;
  int t = threadIdx.x, l = t & 63, w = t >> 6;
  if (t == 0) wait_word_ge(flags, FL_TOPK, 1u, dead);
  __syncthreads();
  for (int e = t; e < 8 * DM; e += 256) {
    int m = e / DM, d = e % DM;
    u32 id = ld_agent(idx + m);
    xs[e] = bf2f(xn[(size_t)id * DM + d]);
  }
  __syncthreads();
  int col = cbk * 64 + l;
  float ag[8], au[8];
#pragma unroll
  for (int m = 0; m < 8; ++m) { ag[m] = 0.f; au[m] = 0.f; }
#pragma unroll 8
  for (int kk = 0; kk < 160; ++kk) {
    int k = w * 160 + kk;
    float g = bf2f(wg[(size_t)k * FF + col]);
    float u = bf2f(wu[(size_t)k * FF + col]);
#pragma unroll
    for (int m = 0; m < 8; ++m) { float xv = xs[m * DM + k]; ag[m] += xv * g; au[m] += xv * u; }
  }
#pragma unroll
  for (int m = 0; m < 8; ++m) { red[((w * 2 + 0) * 8 + m) * 64 + l] = ag[m]; red[((w * 2 + 1) * 8 + m) * 64 + l] = au[m]; }
  __syncthreads();
#pragma unroll
  for (int i = 0; i < 2; ++i) {
    int m = w + 4 * i;
    float g = 0.f, u = 0.f;
#pragma unroll
    for (int q = 0; q < 4; ++q) { g += red[((q * 2 + 0) * 8 + m) * 64 + l]; u += red[((q * 2 + 1) * 8 + m) * 64 + l]; }
    g = rbf(g); u = rbf(u);
    float sg = rbf(g / (1.0f + __expf(-g)));
    st_agent(hmid + m * FF + col, __float_as_uint(rbf(sg * u)));
  }
  DRAIN();
  __syncthreads();
  if (t == 0) add_agent(flags + FL_MOE1, 1u);
}

__device__ __forceinline__ void task_moe2(char* smem, char* ws, const u16* __restrict__ wd, int cb, bool& dead) {
  u32* idx = (u32*)(ws + WS_IDX);
  u32* hmid = (u32*)(ws + WS_HMID);
  u32* upd = (u32*)(ws + WS_UPD);
  const float* r = (const float*)(ws + WS_R);
  u32* flags = (u32*)(ws + WS_FLAG_OFF);
  float* hs = (float*)smem;
  float* red = hs + 8 * FF;
  int t = threadIdx.x, l = t & 63, w = t >> 6;
  if (t == 0) wait_word_ge(flags, FL_MOE1, 27u, dead);
  __syncthreads();
  for (int e = t; e < 8 * FF; e += 256) hs[e] = __uint_as_float(ld_agent(hmid + e));
  __syncthreads();
  int col = cb * 64 + l;
  float a[8];
#pragma unroll
  for (int m = 0; m < 8; ++m) a[m] = 0.f;
#pragma unroll 8
  for (int kk = 0; kk < 432; ++kk) {
    int k = w * 432 + kk;
    float d = bf2f(wd[(size_t)k * DM + col]);
#pragma unroll
    for (int m = 0; m < 8; ++m) a[m] += hs[m * FF + k] * d;
  }
#pragma unroll
  for (int m = 0; m < 8; ++m) red[(w * 8 + m) * 64 + l] = a[m];
  __syncthreads();
#pragma unroll
  for (int i = 0; i < 2; ++i) {
    int m = w + 4 * i;
    float y = red[(0 * 8 + m) * 64 + l] + red[(1 * 8 + m) * 64 + l] + red[(2 * 8 + m) * 64 + l] + red[(3 * 8 + m) * 64 + l];
    y = rbf(y);
    float rv = r[ld_agent(idx + m)];
    float sg = rbf(1.0f / (1.0f + __expf(-rv)));
    st_agent(upd + m * DM + col, __float_as_uint(rbf(y * sg)));
  }
  DRAIN();
  __syncthreads();
  if (t == 0) add_agent(flags + FL_MOE2, 1u);
}

__device__ __forceinline__ void task_attn(char* smem, char* ws, int s, int hg, bool& dead) {
  const u16* qkvx = (const u16*)(ws + WS_QKV);
  const u16* kvm = (const u16*)(ws + WS_KVM);
  u32* flags = (u32*)(ws + WS_FLAG_OFF);
  __amdgpu_buffer_rsrc_t rO = __builtin_amdgcn_make_buffer_rsrc((void*)(ws + WS_OX), 0, S_TOK * HD * 2, 0x00020000);
  int w = threadIdx.x >> 6, l = threadIdx.x & 63, fr = l & 15, g = l >> 4;
  if (w == 0) {
    const int rb = s >> 2;
    int word = (l < 12) ? (FL_TILE + rb * 96 + (l >> 2) * 32 + hg * 4 + (l & 3)) : (FL_STEP + s * 16);
    u32 need = (l < 12) ? 1u : 64u;
    u32 spins = 0;
    for (;;) {
      u32 v = (l < 13) ? ld_agent(flags + word) : 0xffffffffu;
      if (__all(v >= need)) break;
      __builtin_amdgcn_s_sleep(8);
      if (dead || ++spins > 16 * SPIN_CAP) { if (l == 0) atomicOr(flags + FL_ERR, 32u); dead = true; break; }
    }
    __builtin_amdgcn_fence(__ATOMIC_ACQUIRE, "agent");
    DRAIN();
  }
  __syncthreads();
  u16(*Vs)[136] = (u16(*)[136])(smem + w * 64 * 136 * 2);
  int h = hg * 4 + w;
  const u16* Qb = qkvx + (size_t)(s * SEG) * NQKV + h * DH;
  const u16* Kb = Qb + HD;
  const u16* Vb = Qb + 2 * HD;
  const u16* Km = kvm + (size_t)(s * MEMR) * 8192 + h * DH;
  const u16* Vm = Km + HD;
  for (int e = l; e < 64 * 16; e += 64) {
    int j = e >> 4, ch = (e & 15) * 8;
    u32x4 v = {0u, 0u, 0u, 0u};
    if (j < 8) v = *(const u32x4*)(Vm + (size_t)j * 8192 + ch);
    else if (j < 40) v = *(const u32x4*)(Vb + (size_t)(j - 8) * NQKV + ch);
    *(u32x4*)(&Vs[j][ch]) = v;
  }
  f32x4 sc[2][3] = {};
#pragma unroll
  for (int ks = 0; ks < 4; ++ks) {
    bf16x8 a[3], b[2];
#pragma unroll
    for (int t = 0; t < 3; ++t) {
      int j = 16 * t + fr;
      const u16* p = (j < 8) ? (Km + (size_t)j * 8192) : (Kb + (size_t)((j < 40 ? j : 39) - 8) * NQKV);
      a[t] = *(const bf16x8*)(p + 32 * ks + 8 * g);
    }
#pragma unroll
    for (int it = 0; it < 2; ++it) b[it] = *(const bf16x8*)(Qb + (size_t)(16 * it + fr) * NQKV + 32 * ks + 8 * g);
#pragma unroll
    for (int it = 0; it < 2; ++it)
#pragma unroll
      for (int t = 0; t < 3; ++t) sc[it][t] = MFMA16(a[t], b[it], sc[it][t]);
  }
  bf16x8 pb[2][2];
#pragma unroll
  for (int it = 0; it < 2; ++it) {
    int i = 16 * it + fr;
    float pr[3][4];
    float mx = -INFINITY;
#pragma unroll
    for (int t = 0; t < 3; ++t)
#pragma unroll
      for (int r = 0; r < 4; ++r) {
        int j = 16 * t + 4 * g + r;
        float v = rbf(rbf(sc[it][t][r]) * 0.08838834764831845f);
        if (j > 8 + i) v = -INFINITY;
        pr[t][r] = v;
        mx = fmaxf(mx, v);
      }
    mx = red_max_rows(mx);
    float sum = 0.f;
#pragma unroll
    for (int t = 0; t < 3; ++t)
#pragma unroll
      for (int r = 0; r < 4; ++r) { float e = __expf(pr[t][r] - mx); pr[t][r] = e; sum += e; }
    sum = red_sum_rows(sum);
    float inv = 1.0f / sum;
#pragma unroll
    for (int r = 0; r < 4; ++r) {
      pb[it][0][r] = (short)f2bf(pr[0][r] * inv);
      pb[it][0][4 + r] = (short)f2bf(pr[1][r] * inv);
      pb[it][1][r] = (short)f2bf(pr[2][r] * inv);
      pb[it][1][4 + r] = 0;
    }
  }
  __syncthreads();
#pragma unroll
  for (int T = 0; T < 8; ++T) {
    f32x4 oa[2] = {};
#pragma unroll
    for (int u = 0; u < 2; ++u) {
      bf16x8 a;
#pragma unroll
      for (int i = 0; i < 8; ++i) a[i] = (short)Vs[32 * u + 16 * (i >> 2) + 4 * g + (i & 3)][16 * T + fr];
#pragma unroll
      for (int it = 0; it < 2; ++it) oa[it] = MFMA16(a, pb[it][u], oa[it]);
    }
#pragma unroll
    for (int it = 0; it < 2; ++it) {
      u32x2 o;
      o[0] = pack2(oa[it][0], oa[it][1]);
      o[1] = pack2(oa[it][2], oa[it][3]);
      __builtin_amdgcn_raw_buffer_store_b64(o, rO, ((s * SEG + 16 * it + fr) * HD + h * DH + 16 * T + 4 * g) * 2, 0, 16);
    }
  }
  DRAIN();
  __syncthreads();
  if (threadIdx.x == 0) add_agent(flags + FL_ATTN + s * 16, 1u);
}

__device__ __forceinline__ void task_oproj(char* smem, char* ws, const u16* __restrict__ x, u16* __restrict__ out,
                                           int s, int nb, bool& dead) {
  const u16* ox = (const u16*)(ws + WS_OX);
  const u16* Wot = (const u16*)(ws + WS_WOT);
  const u16* xn = (const u16*)(ws + WS_XN);
  const int* idx = (const int*)(ws + WS_IDX);
  const float* upd = (const float*)(ws + WS_UPD);
  u32* flags = (u32*)(ws + WS_FLAG_OFF);
  int w = threadIdx.x >> 6, l = threadIdx.x & 63, fr = l & 15, g = l >> 4;
  if (w == 0) {
    int word = (l == 0) ? (FL_ATTN + s * 16) : FL_MOE2;
    u32 need = (l == 0) ? 8u : 10u;
    u32 spins = 0;
    for (;;) {
      u32 v = (l < 2) ? ld_agent(flags + word) : 0xffffffffu;
      if (__all(v >= need)) break;
      __builtin_amdgcn_s_sleep(8);
      if (dead || ++spins > 16 * SPIN_CAP) { if (l == 0) atomicOr(flags + FL_ERR, 64u); dead = true; break; }
    }
    __builtin_amdgcn_fence(__ATOMIC_ACQUIRE, "agent");
    DRAIN();
  }
  __syncthreads();
  const u16* ap = ox + (size_t)(s * SEG + fr) * HD + w * 1024 + 8 * g;
  const u16* bp = Wot + (size_t)(nb * 64 + fr) * HD + w * 1024 + 8 * g;
  f32x4 acc[2][4] = {};
#pragma unroll 4
  for (int ks = 0; ks < 32; ++ks) {
    bf16x8 a0 = *(const bf16x8*)(ap + 32 * ks);
    bf16x8 a1 = *(const bf16x8*)(ap + (size_t)16 * HD + 32 * ks);
    bf16x8 b[4];
#pragma unroll
    for (int n = 0; n < 4; ++n) b[n] = *(const bf16x8*)(bp + (size_t)(16 * n) * HD + 32 * ks);
#pragma unroll
    for (int n = 0; n < 4; ++n) { acc[0][n] = MFMA16(b[n], a0, acc[0][n]); acc[1][n] = MFMA16(b[n], a1, acc[1][n]); }
  }
  f32x4* red = (f32x4*)smem;
#pragma unroll
  for (int it = 0; it < 2; ++it)
#pragma unroll
    for (int n = 0; n < 4; ++n) red[((w * 2 + it) * 4 + n) * 64 + l] = acc[it][n];
  __syncthreads();
#pragma unroll
  for (int q = 0; q < 2; ++q) {
    int it = w >> 1, n = (w & 1) * 2 + q;
    f32x4 v = (red[((0 * 2 + it) * 4 + n) * 64 + l] + red[((1 * 2 + it) * 4 + n) * 64 + l]) +
              (red[((2 * 2 + it) * 4 + n) * 64 + l] + red[((3 * 2 + it) * 4 + n) * 64 + l]);
    int row = s * SEG + 16 * it + fr, col = nb * 64 + 16 * n + 4 * g;
    int sel = -1;
#pragma unroll
    for (int k = 0; k < 8; ++k) if (idx[k] == row) sel = k;
    u32x2 xv = *(const u32x2*)(x + (size_t)row * DM + col);
    u32x2 nv = *(const u32x2*)(xn + (size_t)row * DM + col);
    u16 res[4];
#pragma unroll
    for (int j = 0; j < 4; ++j) {
      float xx = bf2f((u16)((xv[j >> 1] >> ((j & 1) * 16)) & 0xffff));
      float nn = bf2f((u16)((nv[j >> 1] >> ((j & 1) * 16)) & 0xffff));
      float a = rbf(v[j]);
      float bo = nn;
      if (sel >= 0) bo = rbf(nn + upd[sel * DM + col + j]);
      res[j] = f2bf(rbf(xx + a) + bo);
    }
    u32x2 o;
    o[0] = (u32)res[0] | ((u32)res[1] << 16);
    o[1] = (u32)res[2] | ((u32)res[3] << 16);
    *(u32x2*)(out + (size_t)row * DM + col) = o;
  }
  __syncthreads();
}

#define T_KV 1024
#define T_TOPK (T_KV)
#define T_MOE1 (T_TOPK + 1)
#define T_Q (T_MOE1 + 27)
#define T_MOE2 (T_Q + 512)
#define T_SEG (T_MOE2 + 10)
#define T_END (T_SEG + 18 * NSEG)

__global__ __launch_bounds__(256, 1) void k_main(const u16* __restrict__ x, const u16* __restrict__ mem0,
                                                 const u16* __restrict__ wg, const u16* __restrict__ wu,
                                                 const u16* __restrict__ wd, u16* __restrict__ out, char* ws) {
  extern __shared__ __attribute__((aligned(16))) char smem[];
  const int b = blockIdx.x;
  if (b < N_REC) {
    role_recur(smem, ws, mem0, b);
  } else if (b < N_REC + N_RED) {
    role_reduce(smem, ws, b - N_REC);
  } else {
    bool dead = false;
    for (int id = b - (N_REC + N_RED); id < T_END; id += N_WRK) {
      if (id < T_KV) task_qkv_tile(smem, ws, id >> 6, 32 + (id & 63));
      else if (id < T_MOE1) task_topk(ws);
      else if (id < T_Q) task_moe1(smem, ws, wg, wu, id - T_MOE1, dead);
      else if (id < T_MOE2) task_qkv_tile(smem, ws, (id - T_Q) >> 5, (id - T_Q) & 31);
      else if (id < T_SEG) task_moe2(smem, ws, wd, id - T_MOE2, dead);
      else {
        int q = id - T_SEG, s = q / 18, r = q % 18;
        if (r < 8) task_attn(smem, ws, s, r, dead);
        else task_oproj(smem, ws, x, out, s, r - 8, dead);
      }
      __syncthreads();
    }
  }
}

extern "C" void kernel_launch(void* const* d_in, const int* in_sizes, int n_in, void* d_out, int out_size,
                              void* d_ws, size_t ws_size, hipStream_t stream) {
  const u16* x = (const u16*)d_in[0];
  const u16* mem0 = (const u16*)d_in[1];
  const u16* wq = (const u16*)d_in[2];
  const u16* wk = (const u16*)d_in[3];
  const u16* wv = (const u16*)d_in[4];
  const u16* wo = (const u16*)d_in[5];
  const u16* wr = (const u16*)d_in[8];
  const u16* wg = (const u16*)d_in[9];
  const u16* wu = (const u16*)d_in[10];
  const u16* wd = (const u16*)d_in[11];
  u16* out = (u16*)d_out;
  char* ws = (char*)d_ws;
  k_prep<<<3072 + 165, 256, 0, stream>>>(wq, wk, wv, wo, x, wr, ws);
  (void)hipFuncSetAttribute((const void*)k_main, hipFuncAttributeMaxDynamicSharedMemorySize, LDS_TOTAL);
  k_main<<<N_REC + N_RED + N_WRK, 256, LDS_TOTAL, stream>>>(x, mem0, wg, wu, wd, out, ws);
}
```

```cpp
#include <hip/hip_runtime.h>
#include <hip/hip_bf16.h>
#include <stdint.h>

#define S_TOK 2048
#define DM 640
#define NH 32
#define DH 128
#define HD 4096
#define NQKV 12288
#define MEMR 8
#define SEG 32
#define NSEG 64
#define FF 1728

typedef unsigned short u16;
typedef unsigned int u32;
typedef unsigned long long u64;
typedef __attribute__((ext_vector_type(8))) short bf16x8;
typedef __attribute__((ext_vector_type(4))) short bf16x4;
typedef __attribute__((ext_vector_type(4))) float f32x4;
typedef __attribute__((ext_vector_type(4))) unsigned int u32x4;
typedef __attribute__((ext_vector_type(2))) unsigned int u32x2;

__device__ __forceinline__ float bf2f(u16 v) { return __uint_as_float(((u32)v) << 16); }
__device__ __forceinline__ u16 f2bf(float f) { return __builtin_bit_cast(u16, (__bf16)f); }
__device__ __forceinline__ float rbf(float f) { return bf2f(f2bf(f)); }

#define ZERO4 (f32x4{0.f, 0.f, 0.f, 0.f})
#define MFMA16(a, b, c) __builtin_amdgcn_mfma_f32_16x16x32_bf16((a), (b), (c), 0, 0, 0)

#define WS_P_OFF 0
#define WS_MG_OFF (64 * 5120 * 4)
#define WS_FLAG_OFF (WS_MG_OFF + 2560 * 8)
#define FL_TILE 0
#define FL_STEP 1536
#define FL_ATTN (FL_STEP + 64 * 16)
#define FL_TOPK (FL_ATTN + 64 * 16)
#define FL_MOE1 (FL_TOPK + 16)
#define FL_MOE2 (FL_MOE1 + 16)
#define FL_ERR (FL_MOE2 + 16)
#define FL_WORDS (FL_ERR + 16)
#define WS_SYNC_BYTES (WS_FLAG_OFF + FL_WORDS * 4)
#define WS_ERR_OFF (WS_FLAG_OFF + FL_ERR * 4)
#define WS_WT 0x200000
#define WS_WOT (WS_WT + NQKV * DM * 2)
#define WS_XN (WS_WOT + DM * HD * 2)
#define WS_R (WS_XN + S_TOK * DM * 2)
#define WS_QKV (WS_R + S_TOK * 4)
#define WS_KVM (WS_QKV + (size_t)S_TOK * NQKV * 2)
#define WS_OX (WS_KVM + (size_t)NSEG * MEMR * 8192 * 2)
#define WS_IDX (WS_OX + (size_t)S_TOK * HD * 2)
#define WS_HMID (WS_IDX + 256)
#define WS_UPD (WS_HMID + 8 * FF * 4)

#define N_REC 64
#define N_RED 80
#define N_WRK 112

__global__ __launch_bounds__(256) void k_prep(const u16* __restrict__ wq, const u16* __restrict__ wk,
                                              const u16* __restrict__ wv, const u16* __restrict__ wo,
                                              const u16* __restrict__ x, const u16* __restrict__ wr, char* ws) {
  __shared__ u16 tile[64][66];
  int b = blockIdx.x;
  int t = threadIdx.x;
  if (b < 2560) {
    u16* wt = (u16*)(ws + WS_WT);
    u16* wot = (u16*)(ws + WS_WOT);
    const u16* src;
    u16* dst;
    int C, R, tr, tc;
    if (b < 1920) {
      int which = b / 640, rem = b % 640;
      src = which == 0 ? wq : (which == 1 ? wk : wv);
      dst = wt + (size_t)which * HD * DM;
      R = DM; C = HD;
      tr = rem / 64; tc = rem % 64;
    } else {
      int rem = b - 1920;
      src = wo; dst = wot;
      R = HD; C = DM;
      tr = rem / 10; tc = rem % 10;
    }
#pragma unroll
    for (int i = 0; i < 2; ++i) {
      int r = (t >> 3) + 32 * i, c8 = (t & 7) * 8;
      u32x4 v = *(const u32x4*)(src + (size_t)(tr * 64 + r) * C + tc * 64 + c8);
      u16* p = &tile[r][c8];
#pragma unroll
      for (int j = 0; j < 4; ++j) { p[2 * j] = (u16)(v[j] & 0xffff); p[2 * j + 1] = (u16)(v[j] >> 16); }
    }
    __syncthreads();
#pragma unroll
    for (int i = 0; i < 2; ++i) {
      int c = (t >> 3) + 32 * i, r8 = (t & 7) * 8;
      u32x4 v;
#pragma unroll
      for (int j = 0; j < 4; ++j) v[j] = (u32)tile[r8 + 2 * j][c] | ((u32)tile[r8 + 2 * j + 1][c] << 16);
      *(u32x4*)(dst + (size_t)(tc * 64 + c) * R + tr * 64 + r8) = v;
    }
  } else if (b < 3072) {
    u16* xn = (u16*)(ws + WS_XN);
    float* r = (float*)(ws + WS_R);
    int row = (b - 2560) * 4 + (t >> 6), l = t & 63;
    const u32* xr = (const u32*)(x + (size_t)row * DM);
    float v[10];
    float sum = 0.f;
#pragma unroll
    for (int i = 0; i < 5; ++i) {
      u32 u = xr[l + 64 * i];
      v[2 * i] = bf2f((u16)(u & 0xffff));
      v[2 * i + 1] = bf2f((u16)(u >> 16));
      sum += v[2 * i] + v[2 * i + 1];
    }
#pragma unroll
    for (int o = 32; o > 0; o >>= 1) sum += __shfl_xor(sum, o);
    float mean = sum * (1.0f / DM);
    float sq = 0.f;
#pragma unroll
    for (int i = 0; i < 10; ++i) { float d = v[i] - mean; sq += d * d; }
#pragma unroll
    for (int o = 32; o > 0; o >>= 1) sq += __shfl_xor(sq, o);
    float var = sq * (1.0f / DM);
    float mb = rbf(mean), vb = rbf(var);
    float den = rbf(sqrtf(rbf(vb + 1e-5f)));
    float acc = 0.f;
    u32* xo = (u32*)(xn + (size_t)row * DM);
    const u32* wr2 = (const u32*)wr;
#pragma unroll
    for (int i = 0; i < 5; ++i) {
      u16 a = f2bf(rbf(v[2 * i] - mb) / den);
      u16 bb = f2bf(rbf(v[2 * i + 1] - mb) / den);
      xo[l + 64 * i] = (u32)a | ((u32)bb << 16);
      u32 w = wr2[l + 64 * i];
      acc += bf2f(a) * bf2f((u16)(w & 0xffff)) + bf2f(bb) * bf2f((u16)(w >> 16));
    }
#pragma unroll
    for (int o = 32; o > 0; o >>= 1) acc += __shfl_xor(acc, o);
    if (l == 0) r[row] = rbf(acc);
  } else {
    u32x4 z = {0u, 0u, 0u, 0u};
    int base = (b - 3072) * 512 + t;
#pragma unroll
    for (int i = 0; i < 2; ++i) {
      int e = base + 256 * i;
      if (e < WS_SYNC_BYTES / 16) ((u32x4*)ws)[e] = z;
    }
  }
}

#define MS_STRIDE 648
#define VT_STRIDE 36
#define LDS_VW 0
#define LDS_MEM (LDS_VW + 81920)
#define LDS_SP (LDS_MEM + 10752)
#define LDS_SM (LDS_SP + 4096)
#define LDS_VM (LDS_SM + 1024)
#define LDS_VT (LDS_VM + 1024)
#define LDS_WO (LDS_VT + 4608)
#define LDS_TOTAL 155776

#define SPIN_CAP (1u << 17)

#define MFMA_A0(acc, wa, b) asm("v_mfma_f32_16x16x32_bf16 %0, %1, %2, 0" : "=&v"(acc) : "a"(wa), "v"(b))
#define MFMA_A(acc, wa, b) asm("v_mfma_f32_16x16x32_bf16 %0, %1, %2, %0" : "+v"(acc) : "a"(wa), "v"(b))
#define MFMA_V0(acc, wa, b) asm("v_mfma_f32_16x16x32_bf16 %0, %1, %2, 0" : "=&v"(acc) : "v"(wa), "v"(b))
#define MFMA_V(acc, wa, b) asm("v_mfma_f32_16x16x32_bf16 %0, %1, %2, %0" : "+v"(acc) : "v"(wa), "v"(b))
#define MFMA_K16(acc, wa, b) asm("v_mfma_f32_16x16x16_bf16 %0, %1, %2, %0" : "+v"(acc) : "v"(wa), "v"(b))
#define ROR8(x) __builtin_amdgcn_update_dpp(0u, (x), 0x128, 0xf, 0xf, false)
typedef __attribute__((ext_vector_type(2))) float f32x2;

__device__ __forceinline__ float red_max_rows(float x) {
  u32 u = __float_as_uint(x);
  auto a = __builtin_amdgcn_permlane16_swap(u, u, false, false);
  float m = fmaxf(__uint_as_float(a[0]), __uint_as_float(a[1]));
  u = __float_as_uint(m);
  auto b = __builtin_amdgcn_permlane32_swap(u, u, false, false);
  return fmaxf(__uint_as_float(b[0]), __uint_as_float(b[1]));
}
__device__ __forceinline__ float red_sum_rows(float x) {
  u32 u = __float_as_uint(x);
  auto a = __builtin_amdgcn_permlane16_swap(u, u, false, false);
  float m = __uint_as_float(a[0]) + __uint_as_float(a[1]);
  u = __float_as_uint(m);
  auto b = __builtin_amdgcn_permlane32_swap(u, u, false, false);
  return __uint_as_float(b[0]) + __uint_as_float(b[1]);
}
__device__ __forceinline__ u32 pack2(float a, float b) {
  u32 r;
  asm("v_cvt_pk_bf16_f32 %0, %1, %2" : "=v"(r) : "v"(a), "v"(b));
  return r;
}
__device__ __forceinline__ u32 ld_agent(const u32* p) { return __hip_atomic_load(p, __ATOMIC_RELAXED, __HIP_MEMORY_SCOPE_AGENT); }
__device__ __forceinline__ void st_agent(u32* p, u32 v) { __hip_atomic_store(p, v, __ATOMIC_RELAXED, __HIP_MEMORY_SCOPE_AGENT); }
__device__ __forceinline__ void add_agent(u32* p, u32 v) { __hip_atomic_fetch_add(p, v, __ATOMIC_RELAXED, __HIP_MEMORY_SCOPE_AGENT); }
#define DRAIN() asm volatile("s_waitcnt vmcnt(0)" ::: "memory")

__device__ __forceinline__ void role_recur(char* smem, char* ws, const u16* __restrict__ mem0, int c) {
  u16* vW = (u16*)(smem + LDS_VW);
  u16* memS = (u16*)(smem + LDS_MEM);
  f32x4* SP = (f32x4*)(smem + LDS_SP);
  f32x4* SM = (f32x4*)(smem + LDS_SM);
  u16* VM = (u16*)(smem + LDS_VM);
  u16* VT = (u16*)(smem + LDS_VT);
  u16* woL = (u16*)(smem + LDS_WO);
  const u16* Wt = (const u16*)(ws + WS_WT);
  const u16* Wot = (const u16*)(ws + WS_WOT);
  u32* flags = (u32*)(ws + WS_FLAG_OFF);
  const int h = c >> 1, half = c & 1;

  __amdgpu_buffer_rsrc_t rP = __builtin_amdgcn_make_buffer_rsrc((void*)(ws + WS_P_OFF), 0, 64 * 5120 * 4, 0x00020000);
  __amdgpu_buffer_rsrc_t rM = __builtin_amdgcn_make_buffer_rsrc((void*)(ws + WS_MG_OFF), 0, 2560 * 8, 0x00020000);
  __amdgpu_buffer_rsrc_t rQ = __builtin_amdgcn_make_buffer_rsrc((void*)(ws + WS_QKV), 0, S_TOK * NQKV * 2, 0x00020000);
  __amdgpu_buffer_rsrc_t rK = __builtin_amdgcn_make_buffer_rsrc((void*)(ws + WS_KVM), 0, NSEG * MEMR * 8192 * 2, 0x00020000);

  bool dead = false;
  bf16x8 wa[3][20];
  bf16x8 wv3[20];
  bf16x8 woa[2][2];
  bf16x8 wov[6][2];
  u32x4 kx[2];
  u32x4 vx[4];
  {
    const int tid = threadIdx.x, w = tid >> 6, l = tid & 63, fr = l & 15, g = l >> 4;
#pragma unroll
    for (int t = 0; t < 4; ++t) {
      const u16* wp = Wt + (size_t)((t >> 1) * HD + h * DH + 32 * w + 16 * (t & 1) + fr) * DM + 8 * g;
#pragma unroll
      for (int ks = 0; ks < 20; ++ks) {
        bf16x8 f = *(const bf16x8*)(wp + 32 * ks);
        if (t < 3) wa[t < 3 ? t : 0][ks] = f; else wv3[ks] = f;
      }
    }
    {
      const u16* wp = Wt + (size_t)(2 * HD + h * DH + 64 * half + 16 * w + fr) * DM + 8 * g;
#pragma unroll
      for (int ks = 0; ks < 20; ++ks) *(bf16x8*)(vW + (w * 20 + ks) * 512 + l * 8) = *(const bf16x8*)(wp + 32 * ks);
    }
#pragma unroll
    for (int T = 0; T < 10; ++T) {
      const u16* wp = Wot + (size_t)(160 * w + 16 * T + fr) * HD + h * DH + 64 * half + 4 * g;
#pragma unroll
      for (int u = 0; u < 2; ++u) {
        bf16x4 lo = *(const bf16x4*)(wp + 32 * u);
        bf16x4 hi = *(const bf16x4*)(wp + 32 * u + 16);
        bf16x8 f = __builtin_shufflevector(lo, hi, 0, 1, 2, 3, 4, 5, 6, 7);
        if (T < 2) woa[T < 2 ? T : 0][u] = f;
        else if (T < 8) wov[(T >= 2 && T < 8) ? T - 2 : 0][u] = f;
        else *(bf16x8*)(woL + (w * 4 + (T - 8) * 2 + u) * 512 + l * 8) = f;
      }
    }
  }

  for (int s = -1; s < NSEG; ++s) {
    int tz = threadIdx.x;
    asm volatile("" : "+v"(tz));
    const int tid = tz, w = tz >> 6, l = tz & 63, fr = l & 15, g = l >> 4;
    if (s >= 0) {
      if (s == 0) {
#pragma unroll
        for (int i = 0; i < 5; ++i) {
          int f = 4 * (tid + 256 * i);
          int m = f / DM, col = f % DM;
          *(u32x2*)(memS + m * MS_STRIDE + col) = *(const u32x2*)(mem0 + f);
        }
      } else {
        u32x4 v[5];
        u32 spins = 0;
        for (;;) {
          u32 bad = 0;
#pragma unroll
          for (int i = 0; i < 5; ++i) v[i] = __builtin_amdgcn_raw_buffer_load_b128(rM, (tid + 256 * i) * 16, 0, 16);
#pragma unroll
          for (int i = 0; i < 5; ++i) bad |= (v[i][1] ^ (u32)s) | (v[i][3] ^ (u32)s);
          if (__all(bad == 0)) break;
          asm volatile("" ::: "memory");
          if (dead || ++spins > SPIN_CAP) { if (l == 0) atomicOr(flags + FL_ERR, 1u); dead = true; break; }
        }
#pragma unroll
        for (int i = 0; i < 5; ++i) {
          int f = 4 * (tid + 256 * i);
          int m = f / DM, col = f % DM;
          u32x2 o; o[0] = v[i][0]; o[1] = v[i][2];
          *(u32x2*)(memS + m * MS_STRIDE + col) = o;
        }
      }
      __syncthreads();

      f32x4 acc[5];
      {
        bf16x8 b[3][2], vf[3][2];
        const u16* mp = memS + (fr & 7) * MS_STRIDE + 8 * g;
        const u16* vp = vW + (w * 20) * 512 + l * 8;
#pragma unroll
        for (int i = 0; i < 4; ++i) { b[i >> 1][i & 1] = *(const bf16x8*)(mp + 32 * i); vf[i >> 1][i & 1] = *(const bf16x8*)(vp + 512 * i); }
#pragma unroll
        for (int ch = 0; ch < 10; ++ch) {
          if (ch < 8) {
#pragma unroll
            for (int i = 0; i < 2; ++i) {
              b[(ch + 2) % 3][i] = *(const bf16x8*)(mp + 32 * (2 * ch + 4 + i));
              vf[(ch + 2) % 3][i] = *(const bf16x8*)(vp + 512 * (2 * ch + 4 + i));
            }
          }
          __builtin_amdgcn_sched_barrier(0);
#pragma unroll
          for (int i = 0; i < 2; ++i) {
            const int ks = 2 * ch + i;
            if (ks == 0) {
              MFMA_A0(acc[0], wa[0][0], b[0][0]);
              MFMA_A0(acc[1], wa[1][0], b[0][0]);
              MFMA_A0(acc[2], wa[2][0], b[0][0]);
              MFMA_V0(acc[3], wv3[0], b[0][0]);
              MFMA_V0(acc[4], vf[0][0], b[0][0]);
            } else {
              MFMA_A(acc[0], wa[0][ks], b[ch % 3][i]);
              MFMA_A(acc[1], wa[1][ks], b[ch % 3][i]);
              MFMA_A(acc[2], wa[2][ks], b[ch % 3][i]);
              MFMA_V(acc[3], wv3[ks], b[ch % 3][i]);
              MFMA_V(acc[4], vf[ch % 3][i], b[ch % 3][i]);
            }
          }
          __builtin_amdgcn_sched_barrier(0);
        }
        asm volatile("s_nop 7\n\ts_nop 7" : "+v"(acc[0]), "+v"(acc[1]), "+v"(acc[2]), "+v"(acc[3]), "+v"(acc[4]));
      }
      u32x4 qf, kf;
      u32x2 vo;
      qf[0] = pack2(acc[0][0], acc[0][1]); qf[1] = pack2(acc[0][2], acc[0][3]);
      qf[2] = pack2(acc[1][0], acc[1][1]); qf[3] = pack2(acc[1][2], acc[1][3]);
      kf[0] = pack2(acc[2][0], acc[2][1]); kf[1] = pack2(acc[2][2], acc[2][3]);
      kf[2] = pack2(acc[3][0], acc[3][1]); kf[3] = pack2(acc[3][2], acc[3][3]);
      vo[0] = pack2(acc[4][0], acc[4][1]); vo[1] = pack2(acc[4][2], acc[4][3]);
      if (s + 1 < NSEG) {
        {
          f32x4 sm, sx0, sx1;
          asm volatile("s_nop 1" : "+v"(qf), "+v"(kf));
          MFMA_V0(sx0, kx[0], qf);
          MFMA_V0(sx1, kx[1], qf);
          MFMA_V0(sm, kf, qf);
          __builtin_amdgcn_sched_barrier(0);
          if (fr < 8) {
            VM[(16 * w + 4 * g + 0) * 8 + fr] = (u16)(vo[0] & 0xffff);
            VM[(16 * w + 4 * g + 1) * 8 + fr] = (u16)(vo[0] >> 16);
            VM[(16 * w + 4 * g + 2) * 8 + fr] = (u16)(vo[1] & 0xffff);
            VM[(16 * w + 4 * g + 3) * 8 + fr] = (u16)(vo[1] >> 16);
          }
          asm volatile("s_nop 7\n\ts_nop 7" : "+v"(sm), "+v"(sx0), "+v"(sx1));
          if (fr < 8) {
            SP[((w * 2 + 0) * 4 + g) * 8 + fr] = sx0;
            SP[((w * 2 + 1) * 4 + g) * 8 + fr] = sx1;
            if (g < 2) SM[(w * 2 + g) * 8 + fr] = sm;
          }
        }
        __syncthreads();
        float pr[6];
        u32x2 vm[4];
        {
          const f32x4* sp = SP + ((fr >> 3) * 4 + g) * 8 + (fr & 7);
          const f32x2* smp = (const f32x2*)(SM + (g >> 1) * 8 + (fr & 7)) + (g & 1);
          f32x4 a0 = sp[0], a1 = sp[64], a2 = sp[128], a3 = sp[192];
          f32x2 m0 = smp[0], m1 = smp[32], m2 = smp[64], m3 = smp[96];
#pragma unroll
          for (int T = 0; T < 4; ++T) { vm[T][0] = *(const u32*)(VM + (16 * T + fr) * 8 + 2 * g); vm[T][1] = 0u; }
          f32x4 a = (a0 + a1) + (a2 + a3);
          f32x2 m = (m0 + m1) + (m2 + m3);
          u32 p0 = pack2(a[0], a[1]), p1 = pack2(a[2], a[3]), p2 = pack2(m[0], m[1]);
          p0 = pack2(__uint_as_float(p0 << 16) * 0.08838834764831845f, __uint_as_float(p0 & 0xffff0000u) * 0.08838834764831845f);
          p1 = pack2(__uint_as_float(p1 << 16) * 0.08838834764831845f, __uint_as_float(p1 & 0xffff0000u) * 0.08838834764831845f);
          p2 = pack2(__uint_as_float(p2 << 16) * 0.08838834764831845f, __uint_as_float(p2 & 0xffff0000u) * 0.08838834764831845f);
          pr[0] = __uint_as_float(p0 << 16); pr[1] = __uint_as_float(p0 & 0xffff0000u);
          pr[2] = __uint_as_float(p1 << 16); pr[3] = __uint_as_float(p1 & 0xffff0000u);
          pr[4] = fr < 8 ? __uint_as_float(p2 << 16) : -INFINITY;
          pr[5] = fr < 8 ? __uint_as_float(p2 & 0xffff0000u) : -INFINITY;
        }
        float mx = fmaxf(fmaxf(fmaxf(pr[0], pr[1]), fmaxf(pr[2], pr[3])), fmaxf(pr[4], pr[5]));
        mx = fmaxf(mx, __uint_as_float(ROR8(__float_as_uint(mx))));
        mx = red_max_rows(mx);
        const float nmx = -mx * 1.4426950408889634f;
#pragma unroll
        for (int i = 0; i < 6; ++i) { pr[i] = __builtin_amdgcn_exp2f(fmaf(pr[i], 1.4426950408889634f, nmx)); }
        float sum = ((pr[0] + pr[1]) + (pr[2] + pr[3])) + (pr[4] + pr[5]);
        sum += __uint_as_float(ROR8(__float_as_uint(sum)));
        sum = red_sum_rows(sum);
        float inv = __builtin_amdgcn_rcpf(sum);
        u32x4 pbx;
        u32x2 pbm;
        pbx[0] = pack2(pr[0] * inv, pr[1] * inv); pbx[1] = pack2(pr[2] * inv, pr[3] * inv);
        pbm[0] = pack2(pr[4] * inv, pr[5] * inv); pbm[1] = 0u;
        pbx[2] = ROR8(pbx[0]); pbx[3] = ROR8(pbx[1]);
        bf16x8 ob[2];
        bf16x8 wl[4];
        {
          f32x4 oa[4];
          asm volatile("s_nop 1" : "+v"(pbx), "+v"(pbm));
#pragma unroll
          for (int T = 0; T < 4; ++T) MFMA_V0(oa[T], vx[T], pbx);
#pragma unroll
          for (int T = 0; T < 4; ++T) MFMA_K16(oa[T], vm[T], pbm);
          __builtin_amdgcn_sched_barrier(0);
#pragma unroll
          for (int i = 0; i < 4; ++i) wl[i] = *(const bf16x8*)(woL + (w * 4 + i) * 512 + l * 8);
          asm volatile("s_nop 7\n\ts_nop 7" : "+v"(oa[0]), "+v"(oa[1]), "+v"(oa[2]), "+v"(oa[3]));
          u32x4 t0, t1;
          t0[0] = pack2(oa[0][0], oa[0][1]); t0[1] = pack2(oa[0][2], oa[0][3]);
          t0[2] = pack2(oa[1][0], oa[1][1]); t0[3] = pack2(oa[1][2], oa[1][3]);
          t1[0] = pack2(oa[2][0], oa[2][1]); t1[1] = pack2(oa[2][2], oa[2][3]);
          t1[2] = pack2(oa[3][0], oa[3][1]); t1[3] = pack2(oa[3][2], oa[3][3]);
          ob[0] = __builtin_bit_cast(bf16x8, t0); ob[1] = __builtin_bit_cast(bf16x8, t1);
        }
        const u32 tagp = (u32)(s % 3) + 1u;
        {
          f32x4 pa[10];
          asm volatile("s_nop 1" : "+v"(ob[0]), "+v"(ob[1]));
          MFMA_A0(pa[0], woa[0][0], ob[0]);
          MFMA_A0(pa[1], woa[1][0], ob[0]);
#pragma unroll
          for (int T = 2; T < 8; ++T) MFMA_V0(pa[T], wov[T - 2][0], ob[0]);
          MFMA_V0(pa[8], wl[0], ob[0]);
          MFMA_V0(pa[9], wl[2], ob[0]);
          MFMA_A(pa[0], woa[0][1], ob[1]);
          MFMA_A(pa[1], woa[1][1], ob[1]);
#pragma unroll
          for (int T = 2; T < 8; ++T) MFMA_V(pa[T], wov[T - 2][1], ob[1]);
          MFMA_V(pa[8], wl[1], ob[1]);
          MFMA_V(pa[9], wl[3], ob[1]);
          asm volatile("s_nop 7\n\ts_nop 7" : "+v"(pa[0]), "+v"(pa[1]), "+v"(pa[2]), "+v"(pa[3]), "+v"(pa[4]),
                       "+v"(pa[5]), "+v"(pa[6]), "+v"(pa[7]), "+v"(pa[8]), "+v"(pa[9]));
          const int soff = (c * 5120 + (fr & 7) * DM + 160 * w + 16 * (fr >> 3) + 4 * g) * 4;
#pragma unroll
          for (int T = 0; T < 10; T += 2) {
            u32x4 ov;
#pragma unroll
            for (int r = 0; r < 4; ++r) {
              u32 lo = __float_as_uint(pa[T][r]);
              u32 hi = __builtin_amdgcn_update_dpp(0u, __float_as_uint(pa[T + 1][r]), 0x118, 0xf, 0xf, false);
              u32 v = (fr < 8) ? lo : hi;
              ov[r] = (v & ~3u) | tagp;
            }
            __builtin_amdgcn_raw_buffer_store_b128(ov, rP, soff + 64 * T, 0, 16);
          }
        }
      }
      if (fr < 8) {
        const int kvo = ((s * MEMR + fr) * 8192 + h * DH) * 2;
        if (half == 0) {
          u32x2 k0, k1;
          k0[0] = kf[0]; k0[1] = kf[1]; k1[0] = kf[2]; k1[1] = kf[3];
          __builtin_amdgcn_raw_buffer_store_b64(k0, rK, kvo + (32 * w + 4 * g) * 2, 0, 16);
          __builtin_amdgcn_raw_buffer_store_b64(k1, rK, kvo + (32 * w + 16 + 4 * g) * 2, 0, 16);
        }
        __builtin_amdgcn_raw_buffer_store_b64(vo, rK, kvo + (HD + 64 * half + 16 * w + 4 * g) * 2, 0, 16);
      }
    }
    if (s + 2 < NSEG) {
      if (((s + 1) & 7) == 0) {
        const int rb = (s + 1) >> 3;
        u32 spins = 0;
        while ((ld_agent(flags + FL_TILE + rb * 96 + 32 + h) & ld_agent(flags + FL_TILE + rb * 96 + 64 + h)) == 0) {
          __builtin_amdgcn_s_sleep(1);
          if (dead || ++spins > SPIN_CAP) { if (l == 0) atomicOr(flags + FL_ERR, 8u); dead = true; break; }
        }
      }
      const int pvr = tid >> 3, pvc = (tid & 7) * 8;
      const int ko = (((s + 1) * SEG + fr) * NQKV + HD + h * DH + 32 * w + 4 * g) * 2;
      u32x2 k00 = __builtin_amdgcn_raw_buffer_load_b64(rQ, ko, 0, 16);
      u32x2 k01 = __builtin_amdgcn_raw_buffer_load_b64(rQ, ko + 32, 0, 16);
      u32x2 k10 = __builtin_amdgcn_raw_buffer_load_b64(rQ, ko + 16 * NQKV * 2, 0, 16);
      u32x2 k11 = __builtin_amdgcn_raw_buffer_load_b64(rQ, ko + 16 * NQKV * 2 + 32, 0, 16);
      u32x4 pv = __builtin_amdgcn_raw_buffer_load_b128(rQ, (((s + 1) * SEG + pvr) * NQKV + 2 * HD + h * DH + 64 * half + pvc) * 2, 0, 16);
      kx[0][0] = k00[0]; kx[0][1] = k00[1]; kx[0][2] = k01[0]; kx[0][3] = k01[1];
      kx[1][0] = k10[0]; kx[1][1] = k10[1]; kx[1][2] = k11[0]; kx[1][3] = k11[1];
#pragma unroll
      for (int j = 0; j < 4; ++j) {
        VT[(pvc + 2 * j) * VT_STRIDE + pvr] = (u16)(pv[j] & 0xffff);
        VT[(pvc + 2 * j + 1) * VT_STRIDE + pvr] = (u16)(pv[j] >> 16);
      }
    }
    DRAIN();
    __syncthreads();
    if (s >= 0 && tid == 0) add_agent(flags + FL_STEP + s * 16, 1u);
    if (s + 2 < NSEG) {
#pragma unroll
      for (int T = 0; T < 4; ++T) {
        u32x2 lo = *(const u32x2*)(VT + (16 * T + fr) * VT_STRIDE + 4 * g);
        u32x2 hi = *(const u32x2*)(VT + (16 * T + fr) * VT_STRIDE + 16 + 4 * g);
        vx[T][0] = lo[0]; vx[T][1] = lo[1]; vx[T][2] = hi[0]; vx[T][3] = hi[1];
      }
    }
  }
}

__device__ __forceinline__ void role_reduce(char* smem, char* ws, int r) {
  float* red = (float*)smem;
  u32* flags = (u32*)(ws + WS_FLAG_OFF);
  __amdgpu_buffer_rsrc_t rP = __builtin_amdgcn_make_buffer_rsrc((void*)(ws + WS_P_OFF), 0, 64 * 5120 * 4, 0x00020000);
  __amdgpu_buffer_rsrc_t rM = __builtin_amdgcn_make_buffer_rsrc((void*)(ws + WS_MG_OFF), 0, 2560 * 8, 0x00020000);
  const int tid = threadIdx.x, w = tid >> 6, l = tid & 63;
  const int base = ((16 * w + (l >> 4)) * 5120 + r * 64 + (l & 15) * 4) * 4;
  bool dead = false;
  for (int s = 0; s + 1 < NSEG; ++s) {
    const u32 tagp = (u32)(s % 3) + 1u;
    u32x4 v[4];
    u32 spins = 0;
    for (;;) {
      u32 bad = 0;
#pragma unroll
      for (int i = 0; i < 4; ++i) v[i] = __builtin_amdgcn_raw_buffer_load_b128(rP, base + i * 4 * 5120 * 4, 0, 16);
#pragma unroll
      for (int i = 0; i < 4; ++i) bad |= ((v[i][0] ^ tagp) | (v[i][1] ^ tagp) | (v[i][2] ^ tagp) | (v[i][3] ^ tagp)) & 3u;
      if (__all(bad == 0)) break;
      asm volatile("" ::: "memory");
      if (dead || ++spins > 8 * SPIN_CAP) { if (l == 0) atomicOr(flags + FL_ERR, 2u); dead = true; break; }
    }
    f32x4 a;
#pragma unroll
    for (int q = 0; q < 4; ++q)
      a[q] = red_sum_rows((__uint_as_float(v[0][q]) + __uint_as_float(v[1][q])) + (__uint_as_float(v[2][q]) + __uint_as_float(v[3][q])));
    float* rd = red + (s & 1) * 256;
    if (l < 16) *(f32x4*)(rd + w * 64 + l * 4) = a;
    __syncthreads();
    if (w == 0 && l < 16) {
      f32x4 x = (*(f32x4*)(rd + l * 4) + *(f32x4*)(rd + 64 + l * 4)) + (*(f32x4*)(rd + 128 + l * 4) + *(f32x4*)(rd + 192 + l * 4));
      u32x4 og;
      og[0] = pack2(x[0], x[1]); og[1] = (u32)(s + 1);
      og[2] = pack2(x[2], x[3]); og[3] = (u32)(s + 1);
      __builtin_amdgcn_raw_buffer_store_b128(og, rM, (r * 32 + l * 2) * 8, 0, 16);
    }
  }
}

__device__ __forceinline__ int opaque_tid() { int t = threadIdx.x; asm volatile("" : "+v"(t)); return t; }
__device__ __forceinline__ void wait_word_ge(u32* flags, int word, u32 val, bool& dead) {
  u32 spins = 0;
  while (ld_agent(flags + word) < val) {
    __builtin_amdgcn_s_sleep(8);
    if (dead || ++spins > 16 * SPIN_CAP) { atomicOr(flags + FL_ERR, 16u); dead = true; break; }
  }
}
__device__ __forceinline__ void acquire_all() {
  __builtin_amdgcn_fence(__ATOMIC_ACQUIRE, "agent");
  DRAIN();
  __syncthreads();
}

__device__ __forceinline__ void task_qkv_tile(char* smem, char* ws, int rb, int cb) {
  const int TX = opaque_tid();
  const u16* A = (const u16*)(ws + WS_XN);
  const u16* Bt = (const u16*)(ws + WS_WT);
  u32* flags = (u32*)(ws + WS_FLAG_OFF);
  __amdgpu_buffer_rsrc_t rQ = __builtin_amdgcn_make_buffer_rsrc((void*)(ws + WS_QKV), 0, S_TOK * NQKV * 2, 0x00020000);
  const int wid = TX >> 6, lane = TX & 63, wr = wid >> 1, wc = wid & 1, fr = lane & 15, g = lane >> 4;
  const int lr = lane >> 3, c0 = ((lane & 7) ^ lr) * 8, c1 = ((lane & 7) ^ lr ^ 1) * 8;
  const u16* ga = A + (size_t)(rb * 256 + wid * 64 + lr) * DM;
  const u16* gb = Bt + (size_t)(cb * 128 + 32 * wid + 8 * (lr >> 2) + (lr & 3)) * DM;
  f32x4 acc[8][4];
#pragma unroll
  for (int m = 0; m < 8; ++m)
#pragma unroll
    for (int n = 0; n < 4; ++n) acc[m][n] = ZERO4;
  auto dma = [&](int t, int j) {
    char* sb = smem + (t % 3) * 49152 + lane * 16;
    if (j < 8)
      __builtin_amdgcn_global_load_lds((const u32*)(ga + j * 8 * DM + t * 64 + ((j & 1) ? c1 : c0)), (u32*)(sb + (wid * 8 + j) * 1024), 16, 0, 0);
    else {
      const int q = j - 8;
      __builtin_amdgcn_global_load_lds((const u32*)(gb + (16 * (q & 1) + 4 * (q >> 1)) * DM + t * 64 + ((q & 1) ? c1 : c0)), (u32*)(sb + 32768 + (wid * 4 + q) * 1024), 16, 0, 0);
    }
  };
#pragma unroll
  for (int j = 0; j < 12; ++j) dma(0, j);
#pragma unroll
  for (int j = 0; j < 12; ++j) dma(1, j);
  const int sw = (fr & 7) ^ (fr >> 3);
  const int ro0 = fr * 128 + ((g ^ sw) * 16), ro1 = fr * 128 + (((4 + g) ^ sw) * 16);
  bf16x8 At[2][8], Bf[2][4];
  auto rdf = [&](int i, int q) {
    const char* sa = smem + ((i >> 1) % 3) * 49152 + wr * 16384 + ((i & 1) ? ro1 : ro0);
    const char* sbb = smem + ((i >> 1) % 3) * 49152 + 32768 + wc * 8192 + ((i & 1) ? ro1 : ro0);
    if (q < 4) Bf[i & 1][q] = *(const bf16x8*)(sbb + q * 2048);
    else At[i & 1][q - 4] = *(const bf16x8*)(sa + (q - 4) * 2048);
  };
  asm volatile("s_waitcnt vmcnt(12)\n\ts_barrier" ::: "memory");
#pragma unroll
  for (int j = 0; j < 6; ++j) dma(2, j);
#pragma unroll
  for (int q = 0; q < 12; ++q) rdf(0, q);
#pragma unroll
  for (int i = 0; i < 20; ++i) {
    const int t = (i + 1) >> 1;
    if (i & 1) {
      if (t < 9) asm volatile("s_waitcnt vmcnt(12) lgkmcnt(0)\n\ts_barrier" ::: "memory");
      else if (t == 9) asm volatile("s_waitcnt vmcnt(0) lgkmcnt(0)\n\ts_barrier" ::: "memory");
    }
#pragma unroll
    for (int m = 0; m < 8; ++m) {
      if (i + 1 < 20 && m < 6) { rdf(i + 1, 2 * m); rdf(i + 1, 2 * m + 1); }
      if (t + 2 < 10 && m < 6) dma(t + 2, ((i & 1) ? 0 : 6) + m);
#pragma unroll
      for (int n = 0; n < 4; ++n) acc[m][n] = MFMA16(Bf[i & 1][n], At[i & 1][m], acc[m][n]);
      __builtin_amdgcn_sched_barrier(0);
    }
  }
  {
    char* eb = smem + 49152 + wid * 16384;
#pragma unroll
    for (int m = 0; m < 8; ++m)
#pragma unroll
      for (int p = 0; p < 2; ++p) {
        u32x4 o;
        o[0] = pack2(acc[m][2 * p][0], acc[m][2 * p][1]);
        o[1] = pack2(acc[m][2 * p][2], acc[m][2 * p][3]);
        o[2] = pack2(acc[m][2 * p + 1][0], acc[m][2 * p + 1][1]);
        o[3] = pack2(acc[m][2 * p + 1][2], acc[m][2 * p + 1][3]);
        *(u32x4*)(eb + m * 2048 + (p ? ro1 : ro0)) = o;
      }
    const int ob = ((rb * 256 + wr * 128 + lr) * NQKV + cb * 128 + wc * 64) * 2;
#pragma unroll
    for (int j = 0; j < 16; ++j) {
      u32x4 o = *(const u32x4*)(eb + j * 1024 + lane * 16);
      __builtin_amdgcn_raw_buffer_store_b128(o, rQ, ob + j * 8 * NQKV * 2 + ((j & 1) ? c1 : c0) * 2, 0, 16);
    }
  }
  DRAIN();
  __syncthreads();
  if (TX == 0) st_agent(flags + FL_TILE + rb * 96 + cb, 1u);
}

__device__ __forceinline__ void task_topk(char* ws) {
  const int TX = opaque_tid();
  const float* r = (const float*)(ws + WS_R);
  u32* idx = (u32*)(ws + WS_IDX);
  u32* flags = (u32*)(ws + WS_FLAG_OFF);
  if (TX < 64) {
    int l = TX;
    float v[32];
#pragma unroll
    for (int i = 0; i < 32; ++i) v[i] = r[l + 64 * i];
    u32 mine = 0;
    for (int k = 0; k < 8; ++k) {
      float bv = v[0]; int bi = l;
#pragma unroll
      for (int i = 1; i < 32; ++i) if (v[i] > bv) { bv = v[i]; bi = l + 64 * i; }
#pragma unroll
      for (int o = 32; o > 0; o >>= 1) {
        float ov = __shfl_xor(bv, o); int oi = __shfl_xor(bi, o);
        if (ov > bv || (ov == bv && oi < bi)) { bv = ov; bi = oi; }
      }
      if (l == k) mine = (u32)bi;
#pragma unroll
      for (int i = 0; i < 32; ++i) if (l + 64 * i == bi) v[i] = -INFINITY;
    }
    if (l < 8) st_agent(idx + l, mine);
    DRAIN();
    if (l == 0) st_agent(flags + FL_TOPK, 1u);
  }
}

__device__ __forceinline__ void task_moe1(char* smem, char* ws, const u16* __restrict__ wg, const u16* __restrict__ wu,
                                          int cbk, bool& dead) {
  const int TX = opaque_tid();
  const u16* xn = (const u16*)(ws + WS_XN);
  u32* idx = (u32*)(ws + WS_IDX);
  u32* hmid = (u32*)(ws + WS_HMID);
  u32* flags = (u32*)(ws + WS_FLAG_OFF);
  float* xs = (float*)smem;
  float* red = xs + 8 * DM;
  float* gu = red + 128 * 260;
  const int t = TX, cg = t & 3, kg = t >> 2;
  u32x4 vg[10], vu[10];
#pragma unroll
  for (int i = 0; i < 10; ++i) {
    vg[i] = *(const u32x4*)(wg + (size_t)(kg + 64 * i) * FF + cbk * 32 + cg * 8);
    vu[i] = *(const u32x4*)(wu + (size_t)(kg + 64 * i) * FF + cbk * 32 + cg * 8);
  }
  if (t == 0) wait_word_ge(flags, FL_TOPK, 1u, dead);
  __syncthreads();
  for (int e = t; e < 8 * DM; e += 256) {
    int m = e / DM, d = e % DM;
    u32 id = ld_agent(idx + m) & (S_TOK - 1);
    xs[d * 8 + m] = bf2f(xn[(size_t)id * DM + d]);
  }
  __syncthreads();
  float ag[8][8], au[8][8];
#pragma unroll
  for (int m = 0; m < 8; ++m)
#pragma unroll
    for (int j = 0; j < 8; ++j) { ag[m][j] = 0.f; au[m][j] = 0.f; }
#pragma unroll
  for (int i = 0; i < 10; ++i) {
    f32x4 x0 = *(const f32x4*)(xs + (kg + 64 * i) * 8), x1 = *(const f32x4*)(xs + (kg + 64 * i) * 8 + 4);
#pragma unroll
    for (int j = 0; j < 8; ++j) {
      float gw = __uint_as_float((j & 1) ? (vg[i][j >> 1] & 0xffff0000u) : (vg[i][j >> 1] << 16));
      float uw = __uint_as_float((j & 1) ? (vu[i][j >> 1] & 0xffff0000u) : (vu[i][j >> 1] << 16));
#pragma unroll
      for (int m = 0; m < 8; ++m) {
        float xv = m < 4 ? x0[m & 3] : x1[m & 3];
        ag[m][j] += xv * gw; au[m][j] += xv * uw;
      }
    }
  }
#pragma unroll
  for (int m = 0; m < 8; ++m)
#pragma unroll
    for (int j = 0; j < 8; ++j) { red[(m * 8 + j) * 260 + t] = ag[m][j]; red[(64 + m * 8 + j) * 260 + t] = au[m][j]; }
  __syncthreads();
  {
    const int q = t >> 1, hf = t & 1;
    f32x4 sacc = ZERO4;
#pragma unroll 8
    for (int k = 0; k < 32; ++k) sacc += *(const f32x4*)(red + q * 260 + hf * 128 + k * 4);
#pragma unroll
    for (int c = 0; c < 4; ++c) sacc[c] += __shfl_xor(sacc[c], 1);
    if (hf == 0) {
#pragma unroll
      for (int c = 0; c < 4; ++c) gu[(q >> 3) * 32 + c * 8 + (q & 7)] = sacc[c];
    }
  }
  __syncthreads();
  {
    const int m = t & 7, n = t >> 3;
    float g = rbf(gu[m * 32 + n]), u = rbf(gu[(8 + m) * 32 + n]);
    float sg = rbf(g / (1.0f + __expf(-g)));
    st_agent(hmid + (cbk * 32 + n) * 8 + m, __float_as_uint(rbf(sg * u)));
  }
  DRAIN();
  __syncthreads();
  if (t == 0) add_agent(flags + FL_MOE1, 1u);
}

__device__ __forceinline__ void task_moe2(char* smem, char* ws, const u16* __restrict__ wd, int cb, bool& dead) {
  const int TX = opaque_tid();
  u32* idx = (u32*)(ws + WS_IDX);
  u32* upd = (u32*)(ws + WS_UPD);
  const float* r = (const float*)(ws + WS_R);
  u32* flags = (u32*)(ws + WS_FLAG_OFF);
  __amdgpu_buffer_rsrc_t rH = __builtin_amdgcn_make_buffer_rsrc((void*)(ws + WS_HMID), 0, 8 * FF * 4, 0x00020000);
  float* hs = (float*)smem;
  float* red = hs + 8 * FF;
  float* yo = red + 64 * 260;
  const int t = TX, cg = t & 3, kg = t >> 2;
  u32x4 vd[27];
#pragma unroll
  for (int i = 0; i < 27; ++i) vd[i] = *(const u32x4*)(wd + (size_t)(kg + 64 * i) * DM + cb * 32 + cg * 8);
  if (t == 0) wait_word_ge(flags, FL_MOE1, 54u, dead);
  __syncthreads();
  for (int e = t; e < 2 * FF; e += 256) *(u32x4*)(hs + e * 4) = __builtin_amdgcn_raw_buffer_load_b128(rH, e * 16, 0, 16);
  __syncthreads();
  float a[8][8];
#pragma unroll
  for (int m = 0; m < 8; ++m)
#pragma unroll
    for (int j = 0; j < 8; ++j) a[m][j] = 0.f;
#pragma unroll
  for (int i = 0; i < 27; ++i) {
    f32x4 x0 = *(const f32x4*)(hs + (kg + 64 * i) * 8), x1 = *(const f32x4*)(hs + (kg + 64 * i) * 8 + 4);
#pragma unroll
    for (int j = 0; j < 8; ++j) {
      float dw = __uint_as_float((j & 1) ? (vd[i][j >> 1] & 0xffff0000u) : (vd[i][j >> 1] << 16));
#pragma unroll
      for (int m = 0; m < 8; ++m) a[m][j] += (m < 4 ? x0[m & 3] : x1[m & 3]) * dw;
    }
  }
#pragma unroll
  for (int m = 0; m < 8; ++m)
#pragma unroll
    for (int j = 0; j < 8; ++j) red[(m * 8 + j) * 260 + t] = a[m][j];
  __syncthreads();
  {
    const int q = t >> 2, qf = t & 3;
    f32x4 sacc = ZERO4;
#pragma unroll 8
    for (int k = 0; k < 16; ++k) sacc += *(const f32x4*)(red + q * 260 + qf * 64 + k * 4);
#pragma unroll
    for (int c = 0; c < 4; ++c) { sacc[c] += __shfl_xor(sacc[c], 1); sacc[c] += __shfl_xor(sacc[c], 2); }
    if (qf == 0) {
#pragma unroll
      for (int c = 0; c < 4; ++c) yo[(q >> 3) * 32 + c * 8 + (q & 7)] = sacc[c];
    }
  }
  __syncthreads();
  {
    const int m = t >> 5, n = t & 31;
    float y = rbf(yo[m * 32 + n]);
    float rv = r[ld_agent(idx + m) & (S_TOK - 1)];
    float sg = rbf(1.0f / (1.0f + __expf(-rv)));
    st_agent(upd + m * DM + cb * 32 + n, __float_as_uint(rbf(y * sg)));
  }
  DRAIN();
  __syncthreads();
  if (t == 0) add_agent(flags + FL_MOE2, 1u);
}

__device__ __forceinline__ void task_attn(char* smem, char* ws, int s, int hg, bool& dead) {
  const int TX = opaque_tid();
  const u16* qkvx = (const u16*)(ws + WS_QKV);
  const u16* kvm = (const u16*)(ws + WS_KVM);
  u32* flags = (u32*)(ws + WS_FLAG_OFF);
  __amdgpu_buffer_rsrc_t rO = __builtin_amdgcn_make_buffer_rsrc((void*)(ws + WS_OX), 0, S_TOK * HD * 2, 0x00020000);
  int w = TX >> 6, l = TX & 63, fr = l & 15, g = l >> 4;
  if (w == 0) {
    const int rb = s >> 3;
    int word = FL_TILE + rb * 96 + (l >> 2) * 32 + hg * 4 + (l & 3);
    u32 spins = 0;
    for (;;) {
      u32 v = (l < 12) ? ld_agent(flags + word) : 1u;
      if (__all(v >= 1u)) break;
      __builtin_amdgcn_s_sleep(8);
      if (dead || ++spins > 16 * SPIN_CAP) { if (l == 0) atomicOr(flags + FL_ERR, 32u); dead = true; break; }
    }
    __builtin_amdgcn_fence(__ATOMIC_ACQUIRE, "agent");
    DRAIN();
  }
  __syncthreads();
  u16(*Vs)[136] = (u16(*)[136])(smem + w * 64 * 136 * 2);
  int h = hg * 4 + w;
  const u16* Qb = qkvx + (size_t)(s * SEG) * NQKV + h * DH;
  const u16* Kb = Qb + HD;
  const u16* Vb = Qb + 2 * HD;
  const u16* Km = kvm + (size_t)(s * MEMR) * 8192 + h * DH;
  const u16* Vm = Km + HD;
  bf16x8 a[4][3], b[4][2];
  {
    u32x4 vv[8];
#pragma unroll
    for (int i = 0; i < 8; ++i) vv[i] = *(const u32x4*)(Vb + (size_t)(4 * i + g) * NQKV + fr * 8);
#pragma unroll
    for (int ks = 0; ks < 4; ++ks) {
#pragma unroll
      for (int t = 0; t < 3; ++t) {
        int j = 16 * t + fr;
        j = j < 8 ? 8 : (j < 40 ? j : 39);
        a[ks][t] = *(const bf16x8*)(Kb + (size_t)(j - 8) * NQKV + 32 * ks + 8 * g);
      }
#pragma unroll
      for (int it = 0; it < 2; ++it) b[ks][it] = *(const bf16x8*)(Qb + (size_t)(16 * it + fr) * NQKV + 32 * ks + 8 * g);
    }
#pragma unroll
    for (int i = 0; i < 8; ++i) *(u32x4*)(&Vs[8 + 4 * i + g][fr * 8]) = vv[i];
    u32x4 z = {0u, 0u, 0u, 0u};
#pragma unroll
    for (int i = 0; i < 6; ++i) *(u32x4*)(&Vs[40 + 4 * i + g][fr * 8]) = z;
  }
  if (w == 0) {
    u32 spins = 0;
    while (ld_agent(flags + FL_STEP + s * 16) < 64u) {
      __builtin_amdgcn_s_sleep(2);
      if (dead || ++spins > 64 * SPIN_CAP) { if (l == 0) atomicOr(flags + FL_ERR, 32u); dead = true; break; }
    }
    __builtin_amdgcn_fence(__ATOMIC_ACQUIRE, "agent");
    DRAIN();
  }
  __syncthreads();
  {
    u32x4 vm0 = *(const u32x4*)(Vm + (size_t)g * 8192 + fr * 8);
    u32x4 vm1 = *(const u32x4*)(Vm + (size_t)(4 + g) * 8192 + fr * 8);
    if (fr < 8) {
#pragma unroll
      for (int ks = 0; ks < 4; ++ks) a[ks][0] = *(const bf16x8*)(Km + (size_t)fr * 8192 + 32 * ks + 8 * g);
    }
    *(u32x4*)(&Vs[g][fr * 8]) = vm0;
    *(u32x4*)(&Vs[4 + g][fr * 8]) = vm1;
  }
  f32x4 sc[2][3] = {};
#pragma unroll
  for (int ks = 0; ks < 4; ++ks)
#pragma unroll
    for (int it = 0; it < 2; ++it)
#pragma unroll
      for (int t = 0; t < 3; ++t) sc[it][t] = MFMA16(a[ks][t], b[ks][it], sc[it][t]);
  bf16x8 pb[2][2];
#pragma unroll
  for (int it = 0; it < 2; ++it) {
    int i = 16 * it + fr;
    float pr[3][4];
    float mx = -INFINITY;
#pragma unroll
    for (int t = 0; t < 3; ++t)
#pragma unroll
      for (int r = 0; r < 4; ++r) {
        int j = 16 * t + 4 * g + r;
        float v = rbf(rbf(sc[it][t][r]) * 0.08838834764831845f);
        if (j > 8 + i) v = -INFINITY;
        pr[t][r] = v;
        mx = fmaxf(mx, v);
      }
    mx = red_max_rows(mx);
    float sum = 0.f;
#pragma unroll
    for (int t = 0; t < 3; ++t)
#pragma unroll
      for (int r = 0; r < 4; ++r) { float e = __expf(pr[t][r] - mx); pr[t][r] = e; sum += e; }
    sum = red_sum_rows(sum);
    float inv = 1.0f / sum;
#pragma unroll
    for (int r = 0; r < 4; ++r) {
      pb[it][0][r] = (short)f2bf(pr[0][r] * inv);
      pb[it][0][4 + r] = (short)f2bf(pr[1][r] * inv);
      pb[it][1][r] = (short)f2bf(pr[2][r] * inv);
      pb[it][1][4 + r] = 0;
    }
  }
  __syncthreads();
#pragma unroll
  for (int T = 0; T < 8; ++T) {
    f32x4 oa[2] = {};
#pragma unroll
    for (int u = 0; u < 2; ++u) {
      bf16x8 a;
#pragma unroll
      for (int i = 0; i < 8; ++i) a[i] = (short)Vs[32 * u + 16 * (i >> 2) + 4 * g + (i & 3)][16 * T + fr];
#pragma unroll
      for (int it = 0; it < 2; ++it) oa[it] = MFMA16(a, pb[it][u], oa[it]);
    }
#pragma unroll
    for (int it = 0; it < 2; ++it) {
      u32x2 o;
      o[0] = pack2(oa[it][0], oa[it][1]);
      o[1] = pack2(oa[it][2], oa[it][3]);
      __builtin_amdgcn_raw_buffer_store_b64(o, rO, ((s * SEG + 16 * it + fr) * HD + h * DH + 16 * T + 4 * g) * 2, 0, 16);
    }
  }
  DRAIN();
  __syncthreads();
  if (TX == 0) add_agent(flags + FL_ATTN + s * 16, 1u);
}

__device__ __forceinline__ void task_oproj(char* smem, char* ws, const u16* __restrict__ x, u16* __restrict__ out,
                                           int s, int nb, bool& dead) {
  const int TX = opaque_tid();
  const u16* ox = (const u16*)(ws + WS_OX);
  const u16* Wot = (const u16*)(ws + WS_WOT);
  const u16* xn = (const u16*)(ws + WS_XN);
  const int* idx = (const int*)(ws + WS_IDX);
  const float* upd = (const float*)(ws + WS_UPD);
  u32* flags = (u32*)(ws + WS_FLAG_OFF);
  int w = TX >> 6, l = TX & 63, fr = l & 15, g = l >> 4;
  if (w == 0) {
    int word = (l == 0) ? (FL_ATTN + s * 16) : FL_MOE2;
    u32 need = (l == 0) ? 8u : 20u;
    u32 spins = 0;
    for (;;) {
      u32 v = (l < 2) ? ld_agent(flags + word) : 0xffffffffu;
      if (__all(v >= need)) break;
      __builtin_amdgcn_s_sleep(8);
      if (dead || ++spins > 16 * SPIN_CAP) { if (l == 0) atomicOr(flags + FL_ERR, 64u); dead = true; break; }
    }
    __builtin_amdgcn_fence(__ATOMIC_ACQUIRE, "agent");
    DRAIN();
  }
  __syncthreads();
  f32x4 acc[2][4] = {};
  {
    const int lr = l >> 3, c0 = ((l & 7) ^ lr) * 8, c1 = ((l & 7) ^ lr ^ 1) * 8;
    const u16* ga = ox + (size_t)(s * SEG + lr) * HD + w * 1024;
    const u16* gb = Wot + (size_t)(nb * 64 + lr) * HD + w * 1024;
    char* ring = smem + w * 36864;
    auto stage = [&](int t) {
      char* sb = ring + (t % 3) * 12288 + l * 16;
#pragma unroll
      for (int j = 0; j < 4; ++j)
        __builtin_amdgcn_global_load_lds((const u32*)(ga + (size_t)j * 8 * HD + t * 64 + ((j & 1) ? c1 : c0)), (u32*)(sb + j * 1024), 16, 0, 0);
#pragma unroll
      for (int j = 0; j < 8; ++j)
        __builtin_amdgcn_global_load_lds((const u32*)(gb + (size_t)j * 8 * HD + t * 64 + ((j & 1) ? c1 : c0)), (u32*)(sb + 4096 + j * 1024), 16, 0, 0);
    };
    stage(0); stage(1); stage(2);
    const int sw = (fr & 7) ^ (fr >> 3);
    const int ro0 = fr * 128 + ((g ^ sw) * 16), ro1 = fr * 128 + (((4 + g) ^ sw) * 16);
    bf16x8 fa[2][2], fb[2][4];
    auto rdf = [&](int i) {
      const char* sa = ring + ((i >> 1) % 3) * 12288 + ((i & 1) ? ro1 : ro0);
      fa[i & 1][0] = *(const bf16x8*)(sa);
      fa[i & 1][1] = *(const bf16x8*)(sa + 2048);
#pragma unroll
      for (int n = 0; n < 4; ++n) fb[i & 1][n] = *(const bf16x8*)(sa + 4096 + n * 2048);
    };
    asm volatile("s_waitcnt vmcnt(24)" ::: "memory");
    rdf(0);
#pragma unroll
    for (int i = 0; i < 32; ++i) {
      if (i & 1) {
        const int t = (i + 1) >> 1;
        if (t < 16) {
          asm volatile("s_waitcnt lgkmcnt(0)" ::: "memory");
          if (t + 2 < 16) stage(t + 2);
          if (t < 14) asm volatile("s_waitcnt vmcnt(24)" ::: "memory");
          else if (t == 14) asm volatile("s_waitcnt vmcnt(12)" ::: "memory");
          else asm volatile("s_waitcnt vmcnt(0)" ::: "memory");
        }
      }
      if (i + 1 < 32) rdf(i + 1);
      __builtin_amdgcn_sched_barrier(0);
#pragma unroll
      for (int n = 0; n < 4; ++n) { acc[0][n] = MFMA16(fb[i & 1][n], fa[i & 1][0], acc[0][n]); acc[1][n] = MFMA16(fb[i & 1][n], fa[i & 1][1], acc[1][n]); }
      __builtin_amdgcn_sched_barrier(0);
    }
  }
  __syncthreads();
  f32x4* red = (f32x4*)smem;
#pragma unroll
  for (int it = 0; it < 2; ++it)
#pragma unroll
    for (int n = 0; n < 4; ++n) red[((w * 2 + it) * 4 + n) * 64 + l] = acc[it][n];
  __syncthreads();
#pragma unroll
  for (int q = 0; q < 2; ++q) {
    int it = w >> 1, n = (w & 1) * 2 + q;
    f32x4 v = (red[((0 * 2 + it) * 4 + n) * 64 + l] + red[((1 * 2 + it) * 4 + n) * 64 + l]) +
              (red[((2 * 2 + it) * 4 + n) * 64 + l] + red[((3 * 2 + it) * 4 + n) * 64 + l]);
    int row = s * SEG + 16 * it + fr, col = nb * 64 + 16 * n + 4 * g;
    int sel = -1;
#pragma unroll
    for (int k = 0; k < 8; ++k) if (idx[k] == row) sel = k;
    u32x2 xv = *(const u32x2*)(x + (size_t)row * DM + col);
    u32x2 nv = *(const u32x2*)(xn + (size_t)row * DM + col);
    u16 res[4];
#pragma unroll
    for (int j = 0; j < 4; ++j) {
      float xx = bf2f((u16)((xv[j >> 1] >> ((j & 1) * 16)) & 0xffff));
      float nn = bf2f((u16)((nv[j >> 1] >> ((j & 1) * 16)) & 0xffff));
      float a = rbf(v[j]);
      float bo = nn;
      if (sel >= 0) bo = rbf(nn + upd[sel * DM + col + j]);
      res[j] = f2bf(rbf(xx + a) + bo);
    }
    u32x2 o;
    o[0] = (u32)res[0] | ((u32)res[1] << 16);
    o[1] = (u32)res[2] | ((u32)res[3] << 16);
    *(u32x2*)(out + (size_t)row * DM + col) = o;
  }
  __syncthreads();
}

#define T_A 64
#define T_TOPK T_A
#define T_B (T_TOPK + 1)
#define T_MOE1 (T_B + 128)
#define T_C (T_MOE1 + 54)
#define T_MOE2 (T_C + 128)
#define T_D (T_MOE2 + 20)
#define T_SEG (T_D + 448)
#define T_END (T_SEG + 18 * NSEG)

__global__ __launch_bounds__(256, 1) void k_main(const u16* __restrict__ x, const u16* __restrict__ mem0,
                                                 const u16* __restrict__ wg, const u16* __restrict__ wu,
                                                 const u16* __restrict__ wd, u16* __restrict__ out, char* ws) {
  extern __shared__ __attribute__((aligned(16))) char smem[];
  const int b = blockIdx.x;
  if (b < N_REC) {
    role_recur(smem, ws, mem0, b);
  } else if (b < N_REC + N_RED) {
    role_reduce(smem, ws, b - N_REC);
  } else {
    bool dead = false;
    for (int id = b - (N_REC + N_RED); id < T_END; id += N_WRK) {
      if (id < T_SEG) {
        int rb = -1, cb = 0;
        if (id < T_A) { rb = 0; cb = 32 + id; }
        else if (id < T_B) task_topk(ws);
        else if (id < T_MOE1) { int q = id - T_B; rb = 1 + (q >> 6); cb = 32 + (q & 63); }
        else if (id < T_C) task_moe1(smem, ws, wg, wu, id - T_MOE1, dead);
        else if (id < T_MOE2) { int q = id - T_C; if (q < 64) { rb = q >> 5; cb = q & 31; } else { rb = 3; cb = 32 + (q - 64); } }
        else if (id < T_D) task_moe2(smem, ws, wd, id - T_MOE2, dead);
        else { int q = id - T_D; if (q < 256) { rb = 4 + (q >> 6); cb = 32 + (q & 63); } else { q -= 256; rb = 2 + (q >> 5); cb = q & 31; } }
        if (rb >= 0) task_qkv_tile(smem, ws, rb, cb);
      }
      else {
        int q = id - T_SEG, s = q / 18, r = q % 18;
        if (r < 8) task_attn(smem, ws, s, r, dead);
        else task_oproj(smem, ws, x, out, s, r - 8, dead);
      }
      __syncthreads();
    }
  }
}

extern "C" void kernel_launch(void* const* d_in, const int* in_sizes, int n_in, void* d_out, int out_size,
                              void* d_ws, size_t ws_size, hipStream_t stream) {
  const u16* x = (const u16*)d_in[0];
  const u16* mem0 = (const u16*)d_in[1];
  const u16* wq = (const u16*)d_in[2];
  const u16* wk = (const u16*)d_in[3];
  const u16* wv = (const u16*)d_in[4];
  const u16* wo = (const u16*)d_in[5];
  const u16* wr = (const u16*)d_in[8];
  const u16* wg = (const u16*)d_in[9];
  const u16* wu = (const u16*)d_in[10];
  const u16* wd = (const u16*)d_in[11];
  u16* out = (u16*)d_out;
  char* ws = (char*)d_ws;
  k_prep<<<3072 + 165, 256, 0, stream>>>(wq, wk, wv, wo, x, wr, ws);
  (void)hipFuncSetAttribute((const void*)k_main, hipFuncAttributeMaxDynamicSharedMemorySize, LDS_TOTAL);
  k_main<<<N_REC + N_RED + N_WRK, 256, LDS_TOTAL, stream>>>(x, mem0, wg, wu, wd, out, ws);
}
```

```cpp
#include <hip/hip_runtime.h>
#include <hip/hip_bf16.h>
#include <stdint.h>

#define S_TOK 2048
#define DM 640
#define NH 32
#define DH 128
#define HD 4096
#define NQKV 12288
#define MEMR 8
#define SEG 32
#define NSEG 64
#define FF 1728

typedef unsigned short u16;
typedef unsigned int u32;
typedef unsigned long long u64;
typedef __attribute__((ext_vector_type(8))) short bf16x8;
typedef __attribute__((ext_vector_type(4))) short bf16x4;
typedef __attribute__((ext_vector_type(4))) float f32x4;
typedef __attribute__((ext_vector_type(4))) unsigned int u32x4;
typedef __attribute__((ext_vector_type(2))) unsigned int u32x2;

__device__ __forceinline__ float bf2f(u16 v) { return __uint_as_float(((u32)v) << 16); }
__device__ __forceinline__ u16 f2bf(float f) { return __builtin_bit_cast(u16, (__bf16)f); }
__device__ __forceinline__ float rbf(float f) { return bf2f(f2bf(f)); }

#define ZERO4 (f32x4{0.f, 0.f, 0.f, 0.f})
#define MFMA16(a, b, c) __builtin_amdgcn_mfma_f32_16x16x32_bf16((a), (b), (c), 0, 0, 0)

#define NCOPY 8
#define WS_P_OFF 0
#define WS_MG_OFF (64 * 5120 * 4)
#define WS_FLAG_OFF (WS_MG_OFF + 2560 * 8)
#define FL_TILE 0
#define FL_SM 1024
#define FL_STEP 1536
#define NTW 0
#define NTA(b) ((NTW & (b)) ? 2 : 0)
#define FPC 0
#define QT 0
#define FS_J 16
#define FS_W 128
#define FLS(s, hg, j) (FL_STEP + ((s) * 8 + (hg)) * FS_W + (j) * FS_J)
#define FL_ATTN (FL_STEP + 64 * 8 * FS_W)
#define FL_TOPK (FL_ATTN + 64 * 16)
#define FL_MOE1 (FL_TOPK + 16)
#define FL_MOE2 (FL_MOE1 + 16)
#define FL_ERR (FL_MOE2 + 16)
#define FL_QUIET (FL_ERR + 48)
#define FL_WORDS (FL_ERR + 128)
#define WS_SYNC_BYTES (WS_FLAG_OFF + FL_WORDS * 4)
#define WS_CW_OFF 0xA000000
#define CW_PTR(ws) ((char*)((((uintptr_t)(ws)) + WS_CW_OFF + 0x1FFFFF) & ~(uintptr_t)0x1FFFFF))
#define CW_MEMP 10
#define CW_TS (14 * 0x8000)
#define NCAL 32
#define PB_L NCAL
#define PB_Z (PB_L + 512)
#define MV 1
#define PB_F (PB_Z + ((MV & 1) ? 160 : 320))
#define PB_M (PB_F + (FL_WORDS * 4 + 8191) / 8192)
#define PB_K (PB_M + 32)
#define WS_ERR_OFF (WS_FLAG_OFF + FL_ERR * 4)
#define WS_MG2_OFF 0x8000000
#define WS_WT 0x200000
#define WS_WOT (WS_WT + NQKV * DM * 2)
#define WS_XN (WS_WOT + DM * HD * 2)
#define WS_R (WS_XN + S_TOK * DM * 2)
#define WS_QKV (WS_R + S_TOK * 4)
#define WS_KVM (WS_QKV + (size_t)S_TOK * NQKV * 2)
#define WS_OX (WS_KVM + (size_t)NSEG * MEMR * 8192 * 2)
#define WS_IDX (WS_OX + (size_t)S_TOK * HD * 2)
#define WS_HMID (WS_IDX + 256)
#define WS_UPD (WS_HMID + 8 * FF * 4)
#define WS_CAL (WS_UPD + 8 * DM * 4)

#define N_REC 64
#define CALN 4
#define NDIR 8
#define GATE 2
#define GSL 30
#define GSL2 32
#define NSPL 12
#define FILLW 1
#define NSEN 8
#define SPG 16
#define N_RED 80
#define N_WRK 112

__device__ __forceinline__ float wave_sum(float x) {
  u32 u = __float_as_uint(x);
  auto b = __builtin_amdgcn_permlane32_swap(u, u, false, false);
  x = __uint_as_float(b[0]) + __uint_as_float(b[1]);
  u = __float_as_uint(x);
  auto a = __builtin_amdgcn_permlane16_swap(u, u, false, false);
  x = __uint_as_float(a[0]) + __uint_as_float(a[1]);
  x += __uint_as_float(__builtin_amdgcn_update_dpp(0u, __float_as_uint(x), 0x128, 0xf, 0xf, false));
  x += __uint_as_float(__builtin_amdgcn_update_dpp(0u, __float_as_uint(x), 0x124, 0xf, 0xf, false));
  x += __uint_as_float(__builtin_amdgcn_update_dpp(0u, __float_as_uint(x), 0x4E, 0xf, 0xf, false));
  x += __uint_as_float(__builtin_amdgcn_update_dpp(0u, __float_as_uint(x), 0xB1, 0xf, 0xf, false));
  return x;
}
#define KALL NDIR
#define PB_O (PB_K + ((MV & 2) ? 0 : 16 * KALL))
#define PB_E (PB_O + ((MV & 4) ? 0 : 32 * NSEN))
#define PW 0
__device__ __forceinline__ void pst4(u32x4* p, u32x4 v) {
  *p = v;
}
#define PWX 0
__device__ __forceinline__ void pst1(u32* p, u32 v) {
  *p = v;
}
__global__ __launch_bounds__(256) void k_prep(const u16* __restrict__ x, const u16* __restrict__ wr, char* ws) {
  int b = blockIdx.x;
  int t = threadIdx.x;
  if (b < NCAL) {
    if ((t & 63) == 0) {
      const int j = t >> 6;
      __amdgpu_buffer_rsrc_t rC = __builtin_amdgcn_make_buffer_rsrc((void*)CW_PTR(ws), 0, 0x400000, 0x00020000);
      const u32 xcc = __builtin_amdgcn_s_getreg(20 | (31 << 11)) & 7u;
      const u32 cls = ((xcc >> 2) & 1u) | ((xcc & 1u) << 1);
      const int o = (b >> 4) * 0x200000 + 15 * 0x8000 + j * 8192 + (b & 15) * 128;
      u32 best = 4095u;
#pragma unroll 1
      for (int i = -1; i < CALN; ++i) {
        u64 t0 = __builtin_readcyclecounter();
        __builtin_amdgcn_raw_buffer_store_b32(best, rC, o + (i & 3) * 2048, 0, 16);
        asm volatile("s_waitcnt vmcnt(0)" ::: "memory");
        u64 t1 = __builtin_readcyclecounter();
        if (i >= 0) best = min(best, (u32)(t1 - t0));
      }
      ((u32*)(ws + WS_CAL))[b * 4 + (j ^ cls)] = best;
    }
  } else if (b < PB_Z) {
    u16* xn = (u16*)(ws + WS_XN);
    float* r = (float*)(ws + WS_R);
    int row = (b - PB_L) * 4 + (t >> 6), l = t & 63;
    const u32* xr = (const u32*)(x + (size_t)row * DM);
    float v[10];
    float sum = 0.f;
    u32 wv_[5];
#pragma unroll
    for (int i = 0; i < 5; ++i) wv_[i] = ((const u32*)wr)[l + 64 * i];
#pragma unroll
    for (int i = 0; i < 5; ++i) {
      u32 u = xr[l + 64 * i];
      v[2 * i] = bf2f((u16)(u & 0xffff));
      v[2 * i + 1] = bf2f((u16)(u >> 16));
      sum += v[2 * i] + v[2 * i + 1];
    }
    sum = wave_sum(sum);
    float mean = sum * (1.0f / DM);
    float sq = 0.f;
#pragma unroll
    for (int i = 0; i < 10; ++i) { float d = v[i] - mean; sq += d * d; }
    sq = wave_sum(sq);
    float var = sq * (1.0f / DM);
    float mb = rbf(mean), vb = rbf(var);
    float den = rbf(sqrtf(rbf(vb + 1e-5f)));
    float acc = 0.f;
    u32* xo = (u32*)(xn + (size_t)row * DM);
#pragma unroll
    for (int i = 0; i < 5; ++i) {
      u16 a = f2bf(rbf(v[2 * i] - mb) / den);
      u16 bb = f2bf(rbf(v[2 * i + 1] - mb) / den);
      pst1(xo + l + 64 * i, (u32)a | ((u32)bb << 16));
      u32 w = wv_[i];
      acc += bf2f(a) * bf2f((u16)(w & 0xffff)) + bf2f(bb) * bf2f((u16)(w >> 16));
    }
    acc = wave_sum(acc);
    if (l == 0) r[row] = rbf(acc);
  } else if (b >= PB_O) {
    u32x4 z = {~0u, ~0u, ~0u, ~0u};
    int base = (b - PB_O) * 512 + t;
#pragma unroll
    for (int i = 0; i < 2; ++i) pst4((u32x4*)(ws + WS_OX + (size_t)(NSEG - NSEN) * SEG * HD * 2) + base + 256 * i, z);
  } else if (b >= PB_K) {
    u32x4 z = {~0u, ~0u, ~0u, ~0u};
    int base = (b - PB_K) * 512 + t;
#pragma unroll
    for (int i = 0; i < 2; ++i) pst4((u32x4*)(ws + WS_KVM + (size_t)(NSEG - KALL) * MEMR * 8192 * 2) + base + 256 * i, z);
  } else if (b >= PB_M) {
    u32x4 z = {~0u, ~0u, ~0u, ~0u};
    const int q = b - PB_M;
    u32x4* p = (u32x4*)(CW_PTR(ws) + (1 + (q >> 4)) * 0x80000 + CW_MEMP * 0x8000 + (q & 15) * 8192);
#pragma unroll
    for (int i = 0; i < 2; ++i) pst4(p + t + 256 * i, z);
  } else if (b >= PB_F) {
    u32x4 z = {0u, 0u, 0u, 0u};
    int base = (b - PB_F) * 512 + t;
#pragma unroll
    for (int i = 0; i < 2; ++i) {
      int e = base + 256 * i;
      if (e < FL_WORDS / 4) pst4((u32x4*)(ws + WS_FLAG_OFF) + e, z);
    }
  } else {
    u32x4 z = {0u, 0u, 0u, 0u};
    const int q = b - PB_Z;
    u32x4* p = (u32x4*)(CW_PTR(ws) + (q / 40) * 0x80000 + (q % 40) * 8192);
#pragma unroll
    for (int i = 0; i < 2; ++i) pst4(p + t + 256 * i, z);
  }
}

#define MS_STRIDE 328
#define VT_STRIDE 36
#define QK_STRIDE 136
#define VS_STRIDE 72
#ifndef NVPRE
#define NVPRE 6
#endif
#define LDS_VW 0
#define LDS_MEM (LDS_VW + 81920)
#define LDS_QS (LDS_MEM + 10752)
#define LDS_KS (LDS_QS + 2176)
#define LDS_VS (LDS_KS + 2432)
#define LDS_VM (LDS_VS + 1280)
#define LDS_VT (LDS_VM + 1024)
#define LDS_WO (LDS_VT + 4608)
#define LDS_KX (LDS_WO + 16384)
#define LDS_TOTAL 159744

#define SPIN_CAP (1u << 17)
#define RED_SLEEP 102
#define ALN "6"
#define ALN3 "6"
#define OPA2 16
#define RED_GAP 0
#define CON_SLEEP 36
#define CON_GAP 0
#define WRK_GAP 64
#define WRK_GAPD 32
#define WRK_GAP2 48

#define MFMA_A0(acc, wa, b) asm("v_mfma_f32_16x16x32_bf16 %0, %1, %2, 0" : "=&v"(acc) : "a"(wa), "v"(b))
#define MFMA_A(acc, wa, b) asm("v_mfma_f32_16x16x32_bf16 %0, %1, %2, %0" : "+v"(acc) : "a"(wa), "v"(b))
#define MFMA_V0(acc, wa, b) asm("v_mfma_f32_16x16x32_bf16 %0, %1, %2, 0" : "=&v"(acc) : "v"(wa), "v"(b))
#define MFMA_V(acc, wa, b) asm("v_mfma_f32_16x16x32_bf16 %0, %1, %2, %0" : "+v"(acc) : "v"(wa), "v"(b))
#define MFMB_A0(acc, a, wb) asm("v_mfma_f32_16x16x32_bf16 %0, %1, %2, 0" : "=&v"(acc) : "v"(a), "a"(wb))
#define MFMB_A(acc, a, wb) asm("v_mfma_f32_16x16x32_bf16 %0, %1, %2, %0" : "+v"(acc) : "v"(a), "a"(wb))
#define MFMA_K16(acc, wa, b) asm("v_mfma_f32_16x16x16_bf16 %0, %1, %2, %0" : "+v"(acc) : "v"(wa), "v"(b))
typedef __attribute__((ext_vector_type(16))) short bf16x16;
typedef __attribute__((ext_vector_type(8))) unsigned int u32x8;
#define CAT16(a, b) __builtin_shufflevector((a), (b), 0, 1, 2, 3, 4, 5, 6, 7, 8, 9, 10, 11, 12, 13, 14, 15)
#define SMF_A(acc, a, wb, ix) asm("v_smfmac_f32_16x16x64_bf16 %0, %1, %2, %3" : "+v"(acc) : "v"(a), "a"(wb), "v"(ix))
#define SMF_V(acc, a, wb, ix) asm("v_smfmac_f32_16x16x64_bf16 %0, %1, %2, %3" : "+v"(acc) : "v"(a), "v"(wb), "v"(ix))
#define LDSP(T, off) ((__attribute__((address_space(3))) T*)(u32)(off))
#define ROR8(x) __builtin_amdgcn_update_dpp(0u, (x), 0x128, 0xf, 0xf, false)
typedef __attribute__((ext_vector_type(2))) float f32x2;
__device__ __forceinline__ u32x2 trd(u32 a) {
  return __builtin_bit_cast(u32x2, __builtin_amdgcn_ds_read_tr16_b64_v4i16((__attribute__((address_space(3))) bf16x4*)a));
}
__device__ __forceinline__ bf16x8 trd2(u32 a0, u32 a1) {
  u32x2 x = trd(a0), y = trd(a1);
  return __builtin_bit_cast(bf16x8, __builtin_shufflevector(x, y, 0, 1, 2, 3));
}
__device__ __forceinline__ bf16x16 trd4(u32 a0, u32 a1, u32 a2, u32 a3) {
  u32x2 x = trd(a0), y = trd(a1), z = trd(a2), q = trd(a3);
  u32x4 lo = __builtin_shufflevector(x, y, 0, 1, 2, 3), hi = __builtin_shufflevector(z, q, 0, 1, 2, 3);
  return __builtin_bit_cast(bf16x16, __builtin_shufflevector(lo, hi, 0, 1, 2, 3, 4, 5, 6, 7));
}
#define TRA(dst, a, off) asm volatile("ds_read_b64_tr_b16 %0, %1 offset:%2" : "=v"(dst) : "v"(a), "i"(off))
#define CAT2(x, y) __builtin_shufflevector((x), (y), 0, 1, 2, 3)
#define CAT4(x, y, z, q) __builtin_bit_cast(bf16x16, __builtin_shufflevector(CAT2(x, y), CAT2(z, q), 0, 1, 2, 3, 4, 5, 6, 7))
#define VMW(n) asm volatile("s_waitcnt vmcnt(" #n ")" ::: "memory")

__device__ __forceinline__ float red_max_rows(float x) {
  u32 u = __float_as_uint(x);
  auto a = __builtin_amdgcn_permlane16_swap(u, u, false, false);
  float m = fmaxf(__uint_as_float(a[0]), __uint_as_float(a[1]));
  u = __float_as_uint(m);
  auto b = __builtin_amdgcn_permlane32_swap(u, u, false, false);
  return fmaxf(__uint_as_float(b[0]), __uint_as_float(b[1]));
}
__device__ __forceinline__ float red_sum_rows(float x) {
  u32 u = __float_as_uint(x);
  auto a = __builtin_amdgcn_permlane16_swap(u, u, false, false);
  float m = __uint_as_float(a[0]) + __uint_as_float(a[1]);
  u = __float_as_uint(m);
  auto b = __builtin_amdgcn_permlane32_swap(u, u, false, false);
  return __uint_as_float(b[0]) + __uint_as_float(b[1]);
}
__device__ __forceinline__ u32 pack2(float a, float b) {
  u32 r;
  asm("v_cvt_pk_bf16_f32 %0, %1, %2" : "=v"(r) : "v"(a), "v"(b));
  return r;
}
__device__ __forceinline__ u32 ld_agent(const u32* p) { return __hip_atomic_load(p, __ATOMIC_RELAXED, __HIP_MEMORY_SCOPE_AGENT); }
__device__ __forceinline__ void st_agent(u32* p, u32 v) { __hip_atomic_store(p, v, __ATOMIC_RELAXED, __HIP_MEMORY_SCOPE_AGENT); }
__device__ __forceinline__ void add_agent(u32* p, u32 v) { __hip_atomic_fetch_add(p, v, __ATOMIC_RELAXED, __HIP_MEMORY_SCOPE_AGENT); }
#define DRAIN() asm volatile("s_waitcnt vmcnt(0)" ::: "memory")

__device__ __forceinline__ u32 cal_x(u32 v) {
#pragma unroll
  for (int o = 4; o < 64; o <<= 1) v += __shfl_xor(v, o);
  const u32 a = __builtin_amdgcn_readlane(v, 0), b = __builtin_amdgcn_readlane(v, 1), c = __builtin_amdgcn_readlane(v, 2), d = __builtin_amdgcn_readlane(v, 3);
  const u32 m = min(min(a, b), min(c, d));
  return a == m ? 0u : b == m ? 1u : c == m ? 2u : 3u;
}

#define DT_RED 144
#define DT_CON 232
#define DT_STG 4
#define DT_TS 95
#define D_NOM 124
#define D_SH 2
#define KTS 12
#define DT_KV 200
#define DT_KVL 100
#define DT_OX 550
#define DT_OXM 420
#define DT_OXL 320
#define RS1 30
__device__ __forceinline__ u32 rt_now() { return (u32)__builtin_amdgcn_s_memrealtime(); }
__device__ __forceinline__ int ts_max(u32 st, u32 ref, u32 tag, bool& all) {
  int d = (int)(st - (ref << 2)) >> 2;
  const bool ok = (st & 3u) == tag && d < 400;
  all = __all(ok);
  d = ok ? max(d, -600) : -600;
  d = max(d, (int)__builtin_amdgcn_update_dpp(0u, (u32)d, 0x128, 0xf, 0xf, false));
  d = max(d, (int)__builtin_amdgcn_update_dpp(0u, (u32)d, 0x124, 0xf, 0xf, false));
  d = max(d, (int)__builtin_amdgcn_update_dpp(0u, (u32)d, 0x122, 0xf, 0xf, false));
  d = max(d, (int)__builtin_amdgcn_update_dpp(0u, (u32)d, 0x121, 0xf, 0xf, false));
  return max(max((int)__builtin_amdgcn_readlane((u32)d, 0), (int)__builtin_amdgcn_readlane((u32)d, 16)),
             max((int)__builtin_amdgcn_readlane((u32)d, 32), (int)__builtin_amdgcn_readlane((u32)d, 48)));
}
__device__ __forceinline__ void wait_until(u32 target) {
  for (int i = 0; i < 512; ++i) if ((int)(target - rt_now()) <= 0) break;
}

template <int JS> __device__ __forceinline__ u64 pace_tail(const u32* stp, const u32* done, int dstr, int s, u32 dt, u32& est) {
  const int l = threadIdx.x & 63;
  u64 m = 0;
#pragma unroll 1
  for (int k = 0; k < 48; ++k) {
    const u32 st = ld_agent(stp + (l & 7) * 288);
    if (k == 0) {
      m = __ballot(ld_agent(done + (l >> 3) * dstr + (l & 7) * JS) != 0u);
      if ((m & 0xFFu) == 0xFFu) break;
    }
    bool all;
    const u32 now = rt_now();
    const u32 j = (st - (u32)s) & 3u;
    const int d = ts_max(j < 3u ? st - j * (((DT_CON + 45) << 2) + 1u) : st, now, (u32)(s & 3), all);
    if (all) { est = now + (u32)d + (DT_CON + 40); wait_until(now + (u32)d + dt); break; }
    __builtin_amdgcn_s_sleep(6);
  }
  return m;
}

__device__ __forceinline__ u32x4 ones4() {
  u32 m;
  asm volatile("v_mov_b32 %0, -1" : "=v"(m));
  return u32x4{m, m, m, m};
}
__device__ __forceinline__ void role_recur(char* smem, char* ws, const u16* __restrict__ mem0, const u16* __restrict__ wq,
                                           const u16* __restrict__ wk, const u16* __restrict__ wv, const u16* __restrict__ wo, int c) {
  u16* vW = (u16*)(smem + LDS_VW);
  u16* memS = (u16*)(smem + LDS_MEM);
  u16* QS = (u16*)(smem + LDS_QS);
  u16* KS = (u16*)(smem + LDS_KS);
  u16* VS = (u16*)(smem + LDS_VS);
  u16* VM = (u16*)(smem + LDS_VM);
  u16* VT = (u16*)(smem + LDS_VT);
  u16* woL = (u16*)(smem + LDS_WO);
  u16* KX = (u16*)(smem + LDS_KX);
  u32* flags = (u32*)(ws + WS_FLAG_OFF);
  const int h = (c & 7) | ((c >> 4) << 3), half = (c >> 3) & 1;

  __amdgpu_buffer_rsrc_t rP = __builtin_amdgcn_make_buffer_rsrc((void*)CW_PTR(ws), 0, 0x400000, 0x00020000);
  __amdgpu_buffer_rsrc_t rQ = __builtin_amdgcn_make_buffer_rsrc((void*)(ws + WS_QKV), 0, S_TOK * NQKV * 2, 0x00020000);
  __builtin_amdgcn_global_load_lds((const u32*)(ws + WS_CAL) + (threadIdx.x & 127), (u32*)(smem + 147456 + threadIdx.x * 4), 4, 0, 0);
  {
    const int q = threadIdx.x & 3;
    const char* p = q == 0 ? CW_PTR(ws) + 15 * 0x8000 + 0x1000 : q == 1 ? CW_PTR(ws) + 0x200000 + 15 * 0x8000 + 0x1000
                  : q == 2 ? ws + WS_KVM + 64 : ws + WS_FLAG_OFF + FL_ERR * 4;
    __builtin_amdgcn_global_load_lds((const u32*)p, (u32*)(smem + 158720 + threadIdx.x * 4), 4, 0, 0);
  }
#pragma unroll
  for (int i = 0; i < 3; ++i)
    if (i < 2 || threadIdx.x < 128)
      __builtin_amdgcn_global_load_lds((const u32*)(mem0 + (256 * i + threadIdx.x) * 8), (u32*)(smem + 148480 + (256 * i + threadIdx.x) * 16), 16, 0, 0);
  __amdgpu_buffer_rsrc_t rK = __builtin_amdgcn_make_buffer_rsrc((void*)(ws + WS_KVM), 0, NSEG * MEMR * 8192 * 2, 0x00020000);
  bool dead = false;
  bf16x16 wa[3][10];
  bf16x16 wv3[10];
  bf16x16 woa[2];
  bf16x16 wov[6];
  u32x4 vx[4];
  u32x4 pk0, pk1, pv;
  {
    const int tid = threadIdx.x, w = tid >> 6, l = tid & 63, fr = l & 15, g = l >> 4, tj = fr >> 2, tq = fr & 3;
    const u32 lb0 = (u32)(uintptr_t)((__attribute__((address_space(3))) char*)smem);
    char* areaA = smem + w * 20480;
    char* areaR = smem + 81920 + w * 16384;
    auto slot = [&](int ch) -> char* {
      return ch < 4 ? areaR + ch * 4096 : ch < 9 ? areaA + (ch - 4) * 4096 : ch < 13 ? areaR + (ch - 9) * 4096
           : ch < 18 ? areaA + (ch - 13) * 4096 : areaR + (ch - 18) * 4096;
    };
    const size_t gqk = (size_t)(l >> 2) * HD + h * DH + 32 * w + ((l & 3) ^ (2 * (l >> 5))) * 8;
    auto ld_chunk = [&](int ch) {
      const u16* gp = (ch < 10 ? wq : wk) + gqk + (size_t)(ch % 10) * 64 * HD;
#pragma unroll
      for (int i = 0; i < 4; ++i)
        __builtin_amdgcn_global_load_lds((const u32*)(gp + (size_t)i * 16 * HD), (u32*)(slot(ch) + i * 1024 + l * 16), 16, 0, NTA(2));
    };
    auto ld_v = [&](int p) {
      const u16* gp = wv + (size_t)(160 * w + 32 * p + (l >> 3)) * HD + h * DH + 64 * half;
#pragma unroll
      for (int i = 0; i < 4; ++i)
        __builtin_amdgcn_global_load_lds((const u32*)(gp + (size_t)i * 8 * HD + (((l & 7) ^ (2 * ((l >> 4) & 1)) ^ (4 * (i & 1))) * 8)),
                                         (u32*)(areaA + p * 4096 + i * 1024 + l * 16), 16, 0, NTA(4));
    };
#pragma unroll
    for (int i = 0; i < 20; ++i) {
      const int e = 64 * i + l, row = e / 20, cc = e % 20;
      __builtin_amdgcn_global_load_lds((const u32*)(wo + (size_t)(h * DH + 64 * half + row) * DM + 160 * w + 8 * cc), (u32*)(areaA + i * 1024 + l * 16), 16, 0, NTA(1));
    }
#pragma unroll
    for (int ch = 0; ch < 4; ++ch) ld_chunk(ch);
    VMW(16);
    bf16x16 wlf[2];
    {
      const u32 ab = lb0 + w * 20480 + (16 * (g & 1) + 4 * (g >> 1) + tj) * 320 + 8 * tq;
#pragma unroll
      for (int T = 0; T < 10; ++T) {
        u32x8 f;
        u32x2 cc[4];
#pragma unroll
        for (int q = 0; q < 4; ++q) TRA(cc[q], ab, (32 * (q & 1) + 8 * (q >> 1)) * 320 + 32 * T);
        asm volatile("s_waitcnt lgkmcnt(0)" : "+v"(cc[0]), "+v"(cc[1]), "+v"(cc[2]), "+v"(cc[3]));
#pragma unroll
        for (int q = 0; q < 4; ++q) { f[4 * (q >> 1) + (q & 1)] = cc[q][0]; f[4 * (q >> 1) + 2 + (q & 1)] = cc[q][1]; }
        bf16x16 fb = __builtin_bit_cast(bf16x16, f);
        if (T < 2) { asm volatile("" : "+a"(fb)); woa[T < 2 ? T : 0] = fb; }
        else if (T < 8) wov[(T >= 2 && T < 8) ? T - 2 : 0] = fb;
        else wlf[(T >= 8) ? T - 8 : 0] = fb;
      }
    }
    asm volatile("s_waitcnt lgkmcnt(0)" ::: "memory");
#pragma unroll
    for (int ch = 4; ch < 9; ++ch) ld_chunk(ch);
    const u32 fo0 = (8 * g + tj) * 64 + (((tq >> 1) ^ (2 * (g & 1))) * 16) + 8 * (tq & 1), fo1 = fo0 ^ 32;
    const u32 fA0 = lb0 + w * 20480 + fo0, fA1 = lb0 + w * 20480 + fo1, fR0 = lb0 + 81920 + w * 16384 + fo0, fR1 = lb0 + 81920 + w * 16384 + fo1;
#pragma unroll
    for (int ch = 0; ch < 20; ++ch) {
      if (ch <= 11) VMW(32); else if (ch == 12) VMW(28); else if (ch < 19) VMW(24); else VMW(20);
      const bool inA = (ch >= 4 && ch < 9) || (ch >= 13 && ch < 18);
      const int so = (ch < 4 ? ch : ch < 9 ? ch - 4 : ch < 13 ? ch - 9 : ch < 18 ? ch - 13 : ch - 18) * 4096;
      u32x2 r[8];
#pragma unroll
      for (int q = 0; q < 8; ++q) TRA(r[q], (inA ? ((q & 4) ? fA1 : fA0) : ((q & 4) ? fR1 : fR0)), so + 256 * (q & 1) + 2048 * ((q >> 1) & 1));
      asm volatile("s_waitcnt lgkmcnt(0)" : "+v"(r[0]), "+v"(r[1]), "+v"(r[2]), "+v"(r[3]), "+v"(r[4]), "+v"(r[5]), "+v"(r[6]), "+v"(r[7]) :: "memory");
      bf16x16 f0 = CAT4(r[0], r[1], r[2], r[3]);
      bf16x16 f1 = CAT4(r[4], r[5], r[6], r[7]);
      asm volatile("" : "+a"(f0));
      wa[ch < 10 ? 0 : 2][ch % 10] = f0;
      if (ch < 10) { asm volatile("" : "+a"(f1)); wa[1][ch % 10] = f1; } else wv3[ch % 10] = f1;
      if (ch + 9 < 20) ld_chunk(ch + 9);
      else if (ch >= 13 && ch < 18) ld_v(ch - 13);
    }
    VMW(0);
    __syncthreads();
    {
      u32 spins = 0;
      while ((ld_agent(flags + FL_SM + h) & ld_agent(flags + FL_SM + 32 + h)) == 0) {
        __builtin_amdgcn_s_sleep(1);
        if (dead || ++spins > SPIN_CAP) { if (l == 0) atomicOr(flags + FL_ERR, 4u); dead = true; break; }
      }
      const int ko = (((tid >> 3)) * NQKV + HD + h * DH + (tid & 7) * 16) * 2;
      pk0 = __builtin_amdgcn_raw_buffer_load_b128(rQ, ko, 0, 16);
      pk1 = __builtin_amdgcn_raw_buffer_load_b128(rQ, ko + 16, 0, 16);
      pv = __builtin_amdgcn_raw_buffer_load_b128(rQ, (((tid >> 3)) * NQKV + 2 * HD + h * DH + 64 * half + (tid & 7) * 8) * 2, 0, 16);
    }
    bf16x16 vfr[10];
    {
      const u32 av = lb0 + LDS_VW + (8 * g + tj) * 128 + (((2 * w + (tq >> 1)) ^ (2 * ((tj >> 1) | ((g & 1) << 1)))) * 16) + 8 * (tq & 1);
      const u32 av2 = av + 40960;
#pragma unroll
      for (int t = 0; t < 10; ++t) {
        u32x2 r[4];
#pragma unroll
        for (int q = 0; q < 4; ++q) TRA(r[q], (t < 5 ? av : av2), 8192 * (t % 5) + 512 * (q & 1) + 4096 * (q >> 1));
        asm volatile("s_waitcnt lgkmcnt(0)" : "+v"(r[0]), "+v"(r[1]), "+v"(r[2]), "+v"(r[3]) :: "memory");
        vfr[t] = CAT4(r[0], r[1], r[2], r[3]);
      }
    }
    __syncthreads();
#pragma unroll
    for (int t = 0; t < 10; ++t) {
      *(bf16x8*)(vW + (w * 20 + 2 * t) * 512 + l * 8) = __builtin_shufflevector(vfr[t], vfr[t], 0, 1, 2, 3, 4, 5, 6, 7);
      *(bf16x8*)(vW + (w * 20 + 2 * t + 1) * 512 + l * 8) = __builtin_shufflevector(vfr[t], vfr[t], 8, 9, 10, 11, 12, 13, 14, 15);
    }
#pragma unroll
    for (int i = 0; i < 2; ++i) {
      *(bf16x8*)(woL + (w * 4 + 2 * i) * 512 + l * 8) = __builtin_shufflevector(wlf[i], wlf[i], 0, 1, 2, 3, 4, 5, 6, 7);
      *(bf16x8*)(woL + (w * 4 + 2 * i + 1) * 512 + l * 8) = __builtin_shufflevector(wlf[i], wlf[i], 8, 9, 10, 11, 12, 13, 14, 15);
    }
  }

  __builtin_amdgcn_s_waitcnt(0);
  const u32 X = cal_x(*(const u32*)(smem + 147456 + (threadIdx.x & 63) * 4));
  const u32 XD = 0x200000u | ((cal_x(*(const u32*)(smem + 147456 + 256 + (threadIdx.x & 63) * 4)) ^ X) << 13);
  const u32 xcc = __builtin_amdgcn_s_getreg(20 | (31 << 11)) & 7u;
  const int pbase = (c >> 5) * 0x8000 + (X << 13) + (c & 31) * 256;
  const u32 tbase = CW_TS + (((((xcc >> 2) & 1u) | ((xcc & 1u) << 1)) ^ X) << 13);
  u32 ctgt = 0;
  int davg = (D_NOM + 12) * 8;
  const u32 mbase = (CW_MEMP + 2 * ((xcc >> 1) & 1u)) * 0x8000 + (((((xcc >> 2) & 1u) | ((xcc & 1u) << 1)) ^ X) << 13);
  const u32 lb = (u32)(uintptr_t)((__attribute__((address_space(3))) char*)smem);
  auto prefetch = [&](int ko) {
    pk0 = __builtin_amdgcn_raw_buffer_load_b128(rQ, ko, 0, 16);
    pk1 = __builtin_amdgcn_raw_buffer_load_b128(rQ, ko + 16, 0, 16);
  };
  auto prefetch_v = [&](int vo_) { pv = __builtin_amdgcn_raw_buffer_load_b128(rQ, vo_, 0, 16); };
  auto tail = [&](int s, int tid, u32 fpre) {
    const int l = tid & 63, fr = l & 15, g = l >> 4;
    const int pvr = tid >> 3, pvc = (tid & 7) * 8;
    *(u32x4*)(KX + pvr * QK_STRIDE + 2 * pvc) = pk0;
    *(u32x4*)(KX + pvr * QK_STRIDE + 2 * pvc + 8) = pk1;
#pragma unroll
    for (int j = 0; j < 4; ++j) {
      VT[(pvc + 2 * j) * VT_STRIDE + pvr] = (u16)(pv[j] & 0xffff);
      VT[(pvc + 2 * j + 1) * VT_STRIDE + pvr] = (u16)(pv[j] >> 16);
    }
    asm volatile("s_waitcnt lgkmcnt(0)\n\ts_barrier" ::: "memory");
#pragma unroll
    for (int T = 0; T < 4; ++T) {
      u32x2 lo = *(const u32x2*)(VT + (16 * T + fr) * VT_STRIDE + 4 * g);
      u32x2 hi = *(const u32x2*)(VT + (16 * T + fr) * VT_STRIDE + 16 + 4 * g);
      vx[T][0] = lo[0]; vx[T][1] = lo[1]; vx[T][2] = hi[0]; vx[T][3] = hi[1];
    }
    if (((s + 2) & 7) == 0 || s == 0 || s == 2) {
      const int fw = s == 0 ? FL_SM + 64 + h : FL_TILE + ((s + 2) >> 3) * 96 + 32 + h;
      u32 spins = 0;
      if (!__all(fpre != 0u))
      while ((ld_agent(flags + fw) & ld_agent(flags + fw + 32)) == 0) {
        __builtin_amdgcn_s_sleep(1);
        if (dead || ++spins > SPIN_CAP) { if (l == 0) atomicOr(flags + FL_ERR, 8u); dead = true; break; }
      }
    }
  };
  {
    const int tid = threadIdx.x;
#pragma unroll
    for (int i = 0; i < 5; ++i) {
      const int f = 4 * (tid + 256 * i), m = f / DM, col = f % DM;
      const u32x2 v = *(const u32x2*)(smem + 148480 + f * 2);
      *(u32*)(memS + m * MS_STRIDE + (col >> 1)) = v[0];
      *(u32*)(memS + (m + 8) * MS_STRIDE + (col >> 1)) = v[1];
    }
    tail(-1, tid, 0u);
  }
#pragma unroll 1
  for (int s = 0; s < NSEG; ++s) {
    asm volatile(".p2align " ALN);
    int tz = threadIdx.x;
    asm volatile("" : "+v"(tz));
    const int tid = tz, w = tz >> 6, l = tz & 63, fr = l & 15, g = l >> 4;
    int sidx = fr < 8 ? 0x4444 : 0xEEEE;
    f32x4 acc[5];
#pragma unroll
    for (int i = 0; i < 5; ++i) acc[i] = ZERO4;
    bf16x16 vf[10];
    const u16* vp = vW + (w * 20) * 512 + l * 8;
#define VFR(t) CAT16(*(const bf16x8*)(vp + 1024 * (t)), *(const bf16x8*)(vp + 1024 * (t) + 512))
#pragma unroll
    for (int t = 0; t < NVPRE; ++t) vf[t] = VFR(t);
#pragma unroll
    for (int t = 0; t < NVPRE; ++t) asm volatile("" : "+v"(vf[t]));
    u32 a_ms = lb + LDS_MEM + fr * (MS_STRIDE * 2) + 16 * g;
    const int mv = 4 * (g & 1) + 2 * (g >> 1);
    u32 a_q = lb + LDS_QS + (g & 1) * (8 * QK_STRIDE) + (32 * w + 16 * (g >> 1) + fr) * 2;
    u32 a_qr = lb + LDS_QS + fr * (2 * QK_STRIDE) + 16 * g;
    u32 a_vmr = lb + LDS_VM + (fr * 8 + mv) * 2;
    u32 a_kx = lb + LDS_KX + fr * (2 * QK_STRIDE) + 16 * g;
    u32 a_vm = a_vmr + w * 256;
    u32 a_wo = lb + LDS_WO + tid * 16 + w * 3072;
    u32 a_vs = lb + LDS_VS + (mv * VS_STRIDE + 16 * w + fr) * 2;
    int ko_n = (((s + 1) * SEG + (tid >> 3)) * NQKV + HD + h * DH) * 2 + (tid & 7) * 32;
    int vo_n = (((s + 1) * SEG + (tid >> 3)) * NQKV + 2 * HD + h * DH + 64 * half) * 2 + (tid & 7) * 16;
    asm volatile("" : "+v"(acc[0]), "+v"(acc[1]), "+v"(acc[2]), "+v"(acc[3]), "+v"(acc[4]), "+v"(sidx), "+v"(a_ms));
    asm volatile("" : "+v"(a_q), "+v"(a_qr), "+v"(a_vmr), "+v"(a_kx), "+v"(a_vm), "+v"(a_wo), "+v"(a_vs), "+v"(ko_n), "+v"(vo_n));
    u64 ts64;
    if (s > 0) {
      u32x4 v0, v1;
      u32x2 v2;
      u32 spins = 0;
      u32 a_mw = lb + LDS_MEM + ((4 * (tid & 1) + 8 * ((tid >> 1) & 1)) * MS_STRIDE + 2 * (tid >> 2)) * 2;
      u32 a_mw2 = lb + LDS_MEM + ((2 * (tid & 3) + 8 * ((tid >> 2) & 1)) * MS_STRIDE + 256 + 2 * (tid >> 3)) * 2;
      const u32 sb = ((s & 3) << 19) + (mbase ^ ((s & 1) << 14));
      int go = sb + tid * 16, go2 = sb + 0x8000 + tid * 8;
      asm volatile("" : "+v"(a_mw), "+v"(a_mw2), "+v"(go), "+v"(go2));
      wait_until(ctgt);
      for (;;) {
        v0 = __builtin_amdgcn_raw_buffer_load_b128(rP, go, 0, 16);
        v1 = __builtin_amdgcn_raw_buffer_load_b128(rP, go, 4096, 16);
        v2 = __builtin_amdgcn_raw_buffer_load_b64(rP, go2, 0, 16);
        u32 mxu = max(max(max(v0[0], v0[1]), max(v0[2], v0[3])), max(max(max(v1[0], v1[1]), max(v1[2], v1[3])), max(v2[0], v2[1])));
        if (__all(mxu != ~0u)) break;
        asm volatile("" ::: "memory");
        if (CON_GAP) __builtin_amdgcn_s_sleep(CON_GAP);
        if (dead || ++spins > SPIN_CAP) { if (l == 0) atomicOr(flags + FL_ERR, 1u); dead = true; break; }
      }
#pragma unroll
      for (int j = 0; j < 4; ++j) {
        *LDSP(u32, a_mw + j * (MS_STRIDE * 2)) = v0[j];
        *LDSP(u32, a_mw + 256 + j * (MS_STRIDE * 2)) = v1[j];
      }
      *LDSP(u32, a_mw2) = v2[0];
      *LDSP(u32, a_mw2 + MS_STRIDE * 2) = v2[1];
      DRAIN();
    }
    __syncthreads();
    asm volatile("s_memrealtime %0" : "=s"(ts64));

    bf16x8 kxf[2][4];
    {
      bf16x8 a[3];
      a[0] = *LDSP(const bf16x8, a_ms);
      a[1] = *LDSP(const bf16x8, a_ms + 64);
#define SB __builtin_amdgcn_sched_barrier(0)
#define PIN(x) asm volatile("" : "+v"(x))
#pragma unroll
      for (int t = 0; t < 10; ++t) {
        if (t < 8) a[(t + 2) % 3] = *LDSP(const bf16x8, a_ms + 64 * (t + 2));
        if (t + 3 >= NVPRE && t + 3 < 10) vf[t + 3] = VFR(t + 3);
        if (t >= 6) {
          kxf[0][t >= 6 ? t - 6 : 0] = *LDSP(const bf16x8, a_kx + 64 * (t - 6));
          kxf[1][t >= 6 ? t - 6 : 0] = *LDSP(const bf16x8, a_kx + 32 * QK_STRIDE + 64 * (t - 6));
        }
        SB;
        if (t == 0) { asm volatile("s_waitcnt lgkmcnt(0)" : "+s"(ts64), "+v"(a[0])); SB; }
        SMF_A(acc[0], a[t % 3], wa[0][t], sidx);
        if (t == 1) {
          SB;
          if (tid < 4) {
            __builtin_amdgcn_raw_buffer_store_b32((((u32)ts64 + (u32)((davg >> 3) - D_NOM)) << 2) | (u32)(s & 3), rP, tid * 8192, CW_TS + c * 128, 16);
            if (s > 0 && tid == 0) st_agent(flags + FLS(s - 1, h >> 2, 2 * (h & 3) + half), 1u);
          }
          SB;
        }
        SMF_A(acc[1], a[t % 3], wa[1][t], sidx);
        SMF_A(acc[2], a[t % 3], wa[2][t], sidx);
        SMF_V(acc[3], a[t % 3], wv3[t], sidx);
        SMF_V(acc[4], a[t % 3], vf[t], sidx);
        SB;
      }
    }
    asm volatile("s_nop 3" : "+v"(acc[0]), "+v"(acc[1]), "+v"(acc[2]), "+v"(acc[3]));
    {
      float zq[4], zk[4], zv[2];
#pragma unroll
      for (int r = 0; r < 4; ++r) {
        auto sq = __builtin_amdgcn_permlane32_swap(__float_as_uint(acc[0][r]), __float_as_uint(acc[1][r]), false, false);
        zq[r] = __uint_as_float(sq[0]) + __uint_as_float(sq[1]);
      }
      u32 q01 = pack2(zq[0], zq[1]), q23 = pack2(zq[2], zq[3]);
      *LDSP(u16, a_q) = (u16)(q01 & 0xffff); *LDSP(u16, a_q + 2 * QK_STRIDE) = (u16)(q01 >> 16);
      *LDSP(u16, a_q + 4 * QK_STRIDE) = (u16)(q23 & 0xffff); *LDSP(u16, a_q + 6 * QK_STRIDE) = (u16)(q23 >> 16);
#pragma unroll
      for (int r = 0; r < 4; ++r) {
        auto sk = __builtin_amdgcn_permlane32_swap(__float_as_uint(acc[2][r]), __float_as_uint(acc[3][r]), false, false);
        zk[r] = __uint_as_float(sk[0]) + __uint_as_float(sk[1]);
      }
      u32 k01 = pack2(zk[0], zk[1]), k23 = pack2(zk[2], zk[3]);
      const int kq = LDS_KS - LDS_QS;
      *LDSP(u16, a_q + kq) = (u16)(k01 & 0xffff); *LDSP(u16, a_q + kq + 2 * QK_STRIDE) = (u16)(k01 >> 16);
      *LDSP(u16, a_q + kq + 4 * QK_STRIDE) = (u16)(k23 & 0xffff); *LDSP(u16, a_q + kq + 6 * QK_STRIDE) = (u16)(k23 >> 16);
      asm volatile("" : "+v"(acc[4]));
#pragma unroll
      for (int r = 0; r < 2; ++r) {
        auto sv = __builtin_amdgcn_permlane32_swap(__float_as_uint(acc[4][r]), __float_as_uint(acc[4][r + 2]), false, false);
        zv[r] = __uint_as_float(sv[0]) + __uint_as_float(sv[1]);
      }
      u32 v01 = pack2(zv[0], zv[1]);
      *LDSP(u32, a_vm) = v01;
      *LDSP(u16, a_vs) = (u16)(v01 & 0xffff);
      *LDSP(u16, a_vs + 2 * VS_STRIDE) = (u16)(v01 >> 16);
    }
    __syncthreads();
    u32 st, fpre;
    u64 tc64 = 0;
    if (s + 1 < NSEG) {
      float pr[6];
      u32x2 vm[4];
      int soff;
      {
        bf16x8 qf[4];
#pragma unroll
        for (int t = 0; t < 4; ++t) qf[t] = *LDSP(const bf16x8, a_qr + 64 * t);
        SB;
        f32x4 sm, sx0, sx1;
        MFMA_V0(sx0, kxf[0][0], qf[0]);
        MFMA_V0(sx1, kxf[1][0], qf[0]);
        MFMA_V0(sm, qf[0], qf[0]);
        SB;
        MFMA_V(sx0, kxf[0][1], qf[1]);
        SB;
#pragma unroll
        for (int T = 0; T < 4; ++T) vm[T][0] = *LDSP(const u32, a_vmr + 256 * T);
        SB;
        MFMA_V(sx1, kxf[1][1], qf[1]);
        MFMA_V(sm, qf[1], qf[1]);
        SB;
        prefetch(ko_n);
        SB;
        MFMA_V(sx0, kxf[0][2], qf[2]);
        MFMA_V(sx1, kxf[1][2], qf[2]);
        SB;
        prefetch_v(vo_n);
        SB;
        MFMA_V(sm, qf[2], qf[2]);
        SB;
        {
          int t2 = threadIdx.x;
          asm volatile("" : "+v"(t2));
          const int w2 = t2 >> 6;
          soff = w2 * 0x80000 + ((((t2 >> 3) & 1) | ((t2 >> 4) & 2)) ^ (2 * (w2 & 1))) * 8192 + ((t2 & 7) * 8 + 4 * ((t2 >> 4) & 1)) * 4;
          soff ^= pbase ^ ((s & 1) ? XD : 0u);
          asm volatile("" : "+v"(soff));
          st = __builtin_amdgcn_raw_buffer_load_b32(rP, (t2 & 63) * 128, tbase, 16);
          fpre = ld_agent(flags + (s == 0 ? FL_SM + 64 : FL_TILE + min((s + 2) >> 3, 7) * 96 + 32) + h + (t2 & 32));
        }
        SB;
        MFMA_V(sx1, kxf[1][3], qf[3]);
        MFMA_V(sx0, kxf[0][3], qf[3]);
        MFMA_V(sm, qf[3], qf[3]);
        SB;
#pragma unroll
        for (int T = 0; T < 4; ++T) vm[T][1] = 0u;
        asm volatile("s_nop 5" : "+v"(sm), "+v"(sx0), "+v"(sx1), "+v"(vm[0]), "+v"(vm[1]), "+v"(vm[2]), "+v"(vm[3]));
        f32x4 a;
#pragma unroll
        for (int i = 0; i < 4; ++i)
          a[i] = __uint_as_float(__builtin_amdgcn_update_dpp(__float_as_uint(sx0[i]), __float_as_uint(sx1[i]), 0x128, 0xf, 0xc, false));
        asm volatile("" : "+v"(a), "+v"(sm));
        auto w0 = __builtin_amdgcn_permlane32_swap(__float_as_uint(sm[0]), __float_as_uint(sm[2]), false, false);
        auto w1 = __builtin_amdgcn_permlane32_swap(__float_as_uint(sm[1]), __float_as_uint(sm[3]), false, false);
        const float m0 = __uint_as_float(w0[1]), m1 = __uint_as_float(w1[1]);
        u32 p0 = pack2(a[0], a[1]), p1 = pack2(a[2], a[3]), p2 = pack2(m0, m1);
        p0 = pack2(__uint_as_float(p0 << 16) * 0.08838834764831845f, __uint_as_float(p0 & 0xffff0000u) * 0.08838834764831845f);
        p1 = pack2(__uint_as_float(p1 << 16) * 0.08838834764831845f, __uint_as_float(p1 & 0xffff0000u) * 0.08838834764831845f);
        p2 = pack2(__uint_as_float(p2 << 16) * 0.08838834764831845f, __uint_as_float(p2 & 0xffff0000u) * 0.08838834764831845f);
        pr[0] = __uint_as_float(p0 << 16); pr[1] = __uint_as_float(p0 & 0xffff0000u);
        pr[2] = __uint_as_float(p1 << 16); pr[3] = __uint_as_float(p1 & 0xffff0000u);
        pr[4] = fr < 8 ? __uint_as_float(p2 << 16) : -INFINITY;
        pr[5] = fr < 8 ? __uint_as_float(p2 & 0xffff0000u) : -INFINITY;
      }
      float mx, mtmp;
      asm volatile(
          "v_max3_f32 %0, %2, %3, %4\n\tv_max3_f32 %1, %5, %6, %7\n\tv_max_f32 %0, %0, %1\n\ts_nop 1\n\t"
          "v_max_f32_dpp %0, %0, %0 row_ror:8 row_mask:0xf bank_mask:0xf\n\tv_mov_b32 %1, %0\n\ts_nop 1\n\t"
          "v_permlane16_swap_b32 %0, %1\n\tv_max_f32 %0, %0, %1\n\tv_mov_b32 %1, %0\n\ts_nop 1\n\t"
          "v_permlane32_swap_b32 %0, %1\n\tv_max_f32 %0, %0, %1"
          : "=&v"(mx), "=&v"(mtmp) : "v"(pr[0]), "v"(pr[1]), "v"(pr[2]), "v"(pr[3]), "v"(pr[4]), "v"(pr[5]));
      const float nmx = -mx * 1.4426950408889634f;
#pragma unroll
      for (int i = 0; i < 6; ++i) { pr[i] = __builtin_amdgcn_exp2f(fmaf(pr[i], 1.4426950408889634f, nmx)); }
      float sum = ((pr[0] + pr[1]) + (pr[2] + pr[3])) + (pr[4] + pr[5]);
      sum += __uint_as_float(ROR8(__float_as_uint(sum)));
      sum = red_sum_rows(sum);
      float inv = __builtin_amdgcn_rcpf(sum);
      u32x4 pbx;
      u32x2 pbm;
      pbx[0] = pack2(pr[0] * inv, pr[1] * inv); pbx[1] = pack2(pr[2] * inv, pr[3] * inv);
      pbm[0] = pack2(pr[4] * inv, pr[5] * inv); pbm[1] = 0u;
      asm volatile("s_nop 1" : "+v"(pbx[0]), "+v"(pbx[1]));
      pbx[2] = ROR8(pbx[0]); pbx[3] = ROR8(pbx[1]);
      bf16x8 oS;
      bf16x16 wl[2];
      f32x4 pa[10];
      {
        f32x4 oa[4];
        asm volatile("s_nop 1" : "+v"(pbx), "+v"(pbm));
#pragma unroll
        for (int T = 0; T < 4; ++T) {
          MFMA_V0(oa[T], vx[T], pbx);
          SB;
          if (T < 2) wl[T] = CAT16(*LDSP(const bf16x8, a_wo + 2048 * T), *LDSP(const bf16x8, a_wo + 2048 * T + 1024));
          else { pa[2 * T - 4] = ZERO4; PIN(pa[2 * T - 4]); pa[2 * T - 3] = ZERO4; PIN(pa[2 * T - 3]); }
          SB;
        }
#pragma unroll
        for (int T = 0; T < 4; ++T) {
          MFMA_K16(oa[T], vm[T], pbm);
          SB;
          if (T < 3) { pa[2 * T + 4] = ZERO4; PIN(pa[2 * T + 4]); pa[2 * T + 5] = ZERO4; PIN(pa[2 * T + 5]); }
          SB;
        }
        u32x4 t0, t1;
        asm volatile("" : "+v"(oa[0]), "+v"(oa[1]));
        t0[0] = pack2(oa[0][0], oa[0][1]); t0[1] = pack2(oa[0][2], oa[0][3]);
        t0[2] = pack2(oa[1][0], oa[1][1]); t0[3] = pack2(oa[1][2], oa[1][3]);
        asm volatile("s_nop 3" : "+v"(oa[2]), "+v"(oa[3]), "+v"(t0));
        t1[0] = pack2(oa[2][0], oa[2][1]); t1[1] = pack2(oa[2][2], oa[2][3]);
        t1[2] = pack2(oa[3][0], oa[3][1]); t1[3] = pack2(oa[3][2], oa[3][3]);
        asm volatile("s_nop 1" : "+v"(t0), "+v"(t1));
#pragma unroll
        for (int i = 0; i < 4; ++i) t0[i] = __builtin_amdgcn_update_dpp(t0[i], t1[i], 0x128, 0xf, 0xc, false);
        oS = __builtin_bit_cast(bf16x8, t0);
      }
      const u32 tagp = (u32)(s % 3) + 1u;
      {
        asm volatile("s_nop 1" : "+v"(oS));
#define OPAIR(T) do { \
          if (T == 0) { SMF_A(pa[0], oS, woa[0], sidx); SMF_A(pa[1], oS, woa[1], sidx); } \
          else if (T < 8) { SMF_V(pa[T], oS, wov[(T >= 2 && T < 8) ? T - 2 : 0], sidx); SMF_V(pa[T + 1], oS, wov[(T >= 2 && T < 8) ? T - 1 : 0], sidx); } \
          else { SMF_V(pa[8], oS, wl[0], sidx); SMF_V(pa[9], oS, wl[1], sidx); } \
          SB; } while (0)
#define OSTORE(T) do { u32x4 ov; \
          _Pragma("unroll") for (int r = 0; r < 4; ++r) { \
            auto sw = __builtin_amdgcn_permlane32_swap(__float_as_uint(pa[T][r]), __float_as_uint(pa[T + 1][r]), false, false); \
            ov[r] = (__float_as_uint(__uint_as_float(sw[0]) + __uint_as_float(sw[1])) & ~3u) | tagp; } \
          __builtin_amdgcn_raw_buffer_store_b128(ov, rP, soff, T * 0x8000, 16); \
          SB; } while (0)
        OPAIR(0);
        OPAIR(2);
        asm volatile("" : "+v"(pa[0]), "+v"(pa[1]));
        OPAIR(4);
        asm volatile("s_memrealtime %0" : "=s"(tc64));
        OSTORE(0);
        OPAIR(6);
        asm volatile("" : "+v"(pa[2]), "+v"(pa[3]));
        OSTORE(2);
        OPAIR(8);
        asm volatile("" : "+v"(pa[4]), "+v"(pa[5]));
        OSTORE(4);
        asm volatile("" : "+v"(pa[6]), "+v"(pa[7]));
        OSTORE(6);
        asm volatile("" : "+v"(pa[8]), "+v"(pa[9]));
        OSTORE(8);
      }
    } else {
      asm volatile("" : "=v"(pk0), "=v"(pk1), "=v"(pv), "=v"(st), "=v"(fpre));
    }
    {
      int t3 = threadIdx.x;
      asm volatile("" : "+v"(t3));
      const int tid = t3, w = t3 >> 6, l = t3 & 63;
      if (w < 2) {
        if (half == 0) {
          u32x4 kv = *(const u32x4*)(KS + (tid >> 4) * QK_STRIDE + (tid & 15) * 8);
#pragma unroll
          for (int j = 0; j < 4; ++j) kv[j] = min(kv[j], 0xFFFFFFFEu);
          __builtin_amdgcn_raw_buffer_store_b128(kv, rK, ((s * MEMR + (tid >> 4)) * 8192 + h * DH + (tid & 15) * 8) * 2, 0, 16);
          if ((MV & 2) && s < NDIR)
            __builtin_amdgcn_raw_buffer_store_b128(ones4(), rK, ((s * MEMR + (tid >> 4)) * 8192 + h * DH + (tid & 15) * 8) * 2,
                                                   (NSEG - NDIR) * MEMR * 8192 * 2, 16);
        }
      } else if (w == 2) {
        u32x4 vv = *(const u32x4*)(VS + (l >> 3) * VS_STRIDE + (l & 7) * 8);
#pragma unroll
        for (int j = 0; j < 4; ++j) vv[j] = min(vv[j], 0xFFFFFFFEu);
        __builtin_amdgcn_raw_buffer_store_b128(vv, rK, ((s * MEMR + (l >> 3)) * 8192 + HD + h * DH + 64 * half + (l & 7) * 8) * 2, 0, 16);
        if ((MV & 2) && s < NDIR)
          __builtin_amdgcn_raw_buffer_store_b128(ones4(), rK, ((s * MEMR + (l >> 3)) * 8192 + HD + h * DH + 64 * half + (l & 7) * 8) * 2,
                                                 (NSEG - NDIR) * MEMR * 8192 * 2, 16);
      }
      if (s + 2 < NSEG) tail(s, tid, fpre);
      if (s + 1 < NSEG) {
        bool all;
        int dmx = ts_max(st, (u32)ts64, (u32)(s & 3), all);
#pragma unroll 1
        for (int k = 0; !all && k < KTS; ++k) {
          __builtin_amdgcn_s_sleep(2);
          st = __builtin_amdgcn_raw_buffer_load_b32(rP, l * 128, tbase, 16);
          dmx = ts_max(st, (u32)ts64, (u32)(s & 3), all);
        }
        ctgt = (u32)ts64 + (u32)dmx + DT_CON;
        asm volatile("s_waitcnt lgkmcnt(0)" : "+s"(tc64));
        const int dur = min(max((int)((u32)tc64 - (u32)ts64), D_NOM - 16), D_NOM + 32);
        davg += (dur * 8 - davg) >> D_SH;
      }
    }
  }
  DRAIN();
  __syncthreads();
  if (threadIdx.x == 0) st_agent(flags + FLS(NSEG - 1, h >> 2, 2 * (h & 3) + half), 1u);
}

__device__ __forceinline__ void role_reduce(char* smem, char* ws, int ri) {
  u32* flags = (u32*)(ws + WS_FLAG_OFF);
  __amdgpu_buffer_rsrc_t rP = __builtin_amdgcn_make_buffer_rsrc((void*)CW_PTR(ws), 0, 0x400000, 0x00020000);
  const int tid = threadIdx.x, w = tid >> 6, l = tid & 63;
  const u32 X = cal_x(((const u32*)(ws + WS_CAL))[l]);
  const u32 XD = 0x200000u | ((cal_x(((const u32*)(ws + WS_CAL))[64 + l]) ^ X) << 13);
  const int jj = 2 * (ri >> 3) + ((ri >> 1) & 1);
  const int idx = __builtin_amdgcn_readfirstlane(5 * (jj / 5) + (jj % 5 + w) % 5), r = 4 * idx + (((ri >> 2) & 1) | ((ri & 1) << 1));
  const int base0 = (idx / 5) * 0x80000 + 2 * (idx % 5) * 0x8000 + (((r & 3) ^ X ^ (2 * ((idx / 5) & 1))) << 13) +
                   (l >> 2) * 512 + w * 64 + (l & 3) * 16;
  const int x = (l >> 1) & 7, m = 4 * (l & 1) + (((l >> 4) & 1) << 1) + (l >> 5);
  const int gbase = (CW_MEMP + 2 * ((x >> 1) & 1) + (r >> 6)) * 0x8000 + (((((x >> 2) & 1) | ((x & 1) << 1)) ^ X) << 13) + (r & 63) * 128 + w * 32 + m * 4;
  const u32 xcc = __builtin_amdgcn_s_getreg(20 | (31 << 11)) & 7u;
  const int toff = CW_TS + (((((xcc >> 2) & 1u) | ((xcc & 1u) << 1)) ^ X) << 13) + l * 128;
  u32 anchor = 0;
  bool dead = false;
  {
    const u32 v = __builtin_amdgcn_raw_buffer_load_b32(rP, (l & 1) * 0x200000 + 15 * 0x8000 + 0x1000, 0, 0);
    asm volatile("" :: "v"(v));
  }
  if (MV & 1) {
    const u32x4 z = {0u, 0u, 0u, 0u};
    const int cb = base0 ^ XD;
    __builtin_amdgcn_raw_buffer_store_b128(z, rP, cb, 0, 16);
    __builtin_amdgcn_raw_buffer_store_b128(z, rP, cb + 256, 0, 16);
    __builtin_amdgcn_raw_buffer_store_b128(z, rP, cb + 0x8000, 0, 16);
    __builtin_amdgcn_raw_buffer_store_b128(z, rP, cb + 0x8100, 0, 16);
    DRAIN();
  }
  for (int s = 0; s + 1 < NSEG; ++s) {
    const u32 tagp = (u32)(s % 3) + 1u;
    u32x4 v[4];
    u32 spins = 0;
    int go = (((s + 1) & 3) << 19) + (gbase ^ (((s + 1) & 1) << 14));
    int gf = (((s + 3) & 3) << 19) + (gbase ^ (((s + 3) & 1) << 14));
    int base = base0 ^ ((s & 1) ? XD : 0u), base2 = base + 0x8000;
    asm volatile("" : "+v"(go), "+v"(gf), "+v"(base), "+v"(base2));
    if (s > 0) {
      __builtin_amdgcn_s_sleep(RS1);
      {
        const u32x4 z = {0u, 0u, 0u, 0u};
        const int cb = base ^ XD;
        __builtin_amdgcn_raw_buffer_store_b128(z, rP, cb, 0, 16);
        __builtin_amdgcn_raw_buffer_store_b128(z, rP, cb + 256, 0, 16);
        __builtin_amdgcn_raw_buffer_store_b128(z, rP, cb + 0x8000, 0, 16);
        __builtin_amdgcn_raw_buffer_store_b128(z, rP, cb + 0x8100, 0, 16);
      }
      wait_until(anchor + DT_CON + DT_TS);
    }
    {
#pragma unroll 1
      for (int k = 0; k < (s > 0 ? 64 : 256); ++k) {
        bool all;
        const u32 st = __builtin_amdgcn_raw_buffer_load_b32(rP, toff, 0, 16);
        anchor = rt_now();
        anchor += (u32)ts_max(st, anchor, (u32)(s & 3), all);
        if (all) break;
        if (s > 0) __builtin_amdgcn_s_sleep(2); else __builtin_amdgcn_s_sleep(8);
      }
      wait_until(anchor + DT_RED + DT_STG * (idx % 5));
    }
    asm volatile(".p2align " ALN3);
    for (;;) {
      v[0] = __builtin_amdgcn_raw_buffer_load_b128(rP, base, 0, 16);
      v[1] = __builtin_amdgcn_raw_buffer_load_b128(rP, base + 256, 0, 16);
      v[2] = __builtin_amdgcn_raw_buffer_load_b128(rP, base2, 0, 16);
      v[3] = __builtin_amdgcn_raw_buffer_load_b128(rP, base2 + 256, 0, 16);
      u32 bad = 0;
#pragma unroll
      for (int i = 0; i < 4; ++i)
        bad |= __builtin_amdgcn_bitop3_b32(v[i][0], v[i][1], tagp, 0x7E) | __builtin_amdgcn_bitop3_b32(v[i][2], v[i][3], tagp, 0x7E);
      if (__all((bad & 3u) == 0)) break;
      asm volatile("" ::: "memory");
      if (RED_GAP) __builtin_amdgcn_s_sleep(RED_GAP);
      if (dead || ++spins > 8 * SPIN_CAP) { if (l == 0) atomicOr(flags + FL_ERR, 2u); dead = true; break; }
    }
    float a[4];
#pragma unroll
    for (int q = 0; q < 4; ++q)
      a[q] = (__uint_as_float(v[0][q]) + __uint_as_float(v[1][q])) + (__uint_as_float(v[2][q]) + __uint_as_float(v[3][q]));
    auto s0 = __builtin_amdgcn_permlane32_swap(__float_as_uint(a[0]), __float_as_uint(a[1]), false, false);
    auto s1 = __builtin_amdgcn_permlane32_swap(__float_as_uint(a[2]), __float_as_uint(a[3]), false, false);
    const float b0 = __uint_as_float(s0[0]) + __uint_as_float(s0[1]), b1 = __uint_as_float(s1[0]) + __uint_as_float(s1[1]);
    auto s2 = __builtin_amdgcn_permlane16_swap(__float_as_uint(b0), __float_as_uint(b1), false, false);
    float d = __uint_as_float(s2[0]) + __uint_as_float(s2[1]);
    d += __uint_as_float(__builtin_amdgcn_update_dpp(0u, __float_as_uint(d), 0x128, 0xf, 0xf, false));
    d += __uint_as_float(__builtin_amdgcn_update_dpp(0u, __float_as_uint(d), 0x124, 0xf, 0xf, false));
    const float f0 = __uint_as_float(__builtin_amdgcn_update_dpp(0u, __float_as_uint(d), 0x44, 0xf, 0xf, false));
    const float f1 = __uint_as_float(__builtin_amdgcn_update_dpp(0u, __float_as_uint(d), 0xEE, 0xf, 0xf, false));
    __builtin_amdgcn_raw_buffer_store_b32(min(pack2(f0, f1), 0xFFFFFFFEu), rP, go, 0, 16);
    if (s + 3 < NSEG) __builtin_amdgcn_raw_buffer_store_b32(~0u, rP, gf, 0, 16);
  }
}

__device__ __forceinline__ int opaque_tid() { int t = threadIdx.x; asm volatile("" : "+v"(t)); return t; }
__device__ __forceinline__ void wait_word_ge(u32* flags, int word, u32 val, bool& dead) {
  u32 spins = 0;
  while (ld_agent(flags + word) < val) {
    __builtin_amdgcn_s_sleep(WRK_GAP2);
    if (dead || ++spins > 16 * SPIN_CAP) { atomicOr(flags + FL_ERR, 16u); dead = true; break; }
  }
}
__device__ __forceinline__ void acquire_all() {
  __builtin_amdgcn_fence(__ATOMIC_ACQUIRE, "agent");
  DRAIN();
  __syncthreads();
}

__device__ __forceinline__ void task_qkv_tile(char* smem, char* ws, const u16* __restrict__ Wsel, int rb, int cb) {
  const int TX = opaque_tid();
  const u16* A = (const u16*)(ws + WS_XN);
  u32* flags = (u32*)(ws + WS_FLAG_OFF);
  __amdgpu_buffer_rsrc_t rQ = __builtin_amdgcn_make_buffer_rsrc((void*)(ws + WS_QKV), 0, S_TOK * NQKV * 2, 0x00020000);
  const int wid = TX >> 6, lane = TX & 63, wr = wid >> 1, wc = wid & 1, fr = lane & 15, g = lane >> 4;
  const int lr = lane >> 3, c0 = ((lane & 7) ^ lr) * 8, c1 = ((lane & 7) ^ lr ^ 1) * 8;
  const u16* ga = A + (size_t)(rb * 256 + wid * 64 + lr) * DM;
  const u16* gb = Wsel + (size_t)(16 * wid + (lane >> 4)) * HD + (cb & 31) * 128;
  const int cbA = ((lane & 15) ^ (2 * (lane >> 4))) * 8, cbB = cbA ^ 64;
  f32x4 acc[8][4];
#pragma unroll
  for (int m = 0; m < 8; ++m)
#pragma unroll
    for (int n = 0; n < 4; ++n) acc[m][n] = ZERO4;
  auto dma = [&](int t, int j) {
    char* sb = smem + (t % 3) * 49152 + lane * 16;
    if (j < 8)
      __builtin_amdgcn_global_load_lds((const u32*)(ga + j * 8 * DM + t * 64 + ((j & 1) ? c1 : c0)), (u32*)(sb + (wid * 8 + j) * 1024), 16, 0, 0);
    else {
      const int q = j - 8;
      __builtin_amdgcn_global_load_lds((const u32*)(gb + (size_t)(t * 64 + 4 * q) * HD + ((q >> 1) ? cbB : cbA)), (u32*)(sb + 32768 + (wid * 4 + q) * 1024), 16, 0, NTA(16));
    }
  };
#pragma unroll
  for (int j = 0; j < 12; ++j) dma(0, j);
#pragma unroll
  for (int j = 0; j < 12; ++j) dma(1, j);
  const int sw = (fr & 7) ^ (fr >> 3);
  const int ro0 = fr * 128 + ((g ^ sw) * 16), ro1 = fr * 128 + (((4 + g) ^ sw) * 16);
  bf16x8 At[2][8];
  u32x2 Bl[2][4][2];
  u32 bT[4];
#pragma unroll
  for (int T = 0; T < 4; ++T)
    bT[T] = (u32)(uintptr_t)((__attribute__((address_space(3))) char*)smem) + 32768 + (8 * g + (fr >> 2)) * 256 +
            (((8 * wc + 2 * T + ((fr >> 1) & 1)) ^ (2 * (fr >> 2) + 8 * (g & 1))) * 16) + 8 * (fr & 1);
  u32 bT2[4];
#pragma unroll
  for (int T = 0; T < 4; ++T) bT2[T] = bT[T] + 98304;
  auto rdf = [&](int i, int q) {
    const char* sa = smem + ((i >> 1) % 3) * 49152 + wr * 16384 + ((i & 1) ? ro1 : ro0);
    const int st = (i >> 1) % 3;
    if (q < 4) {
      if (st == 0) { TRA(Bl[i & 1][q][0], bT[q], (i & 1) * 8192); TRA(Bl[i & 1][q][1], bT[q], (i & 1) * 8192 + 1024); }
      else if (st == 1) { TRA(Bl[i & 1][q][0], bT[q], 49152 + (i & 1) * 8192); TRA(Bl[i & 1][q][1], bT[q], 49152 + (i & 1) * 8192 + 1024); }
      else { TRA(Bl[i & 1][q][0], bT2[q], (i & 1) * 8192); TRA(Bl[i & 1][q][1], bT2[q], (i & 1) * 8192 + 1024); }
    }
    else At[i & 1][q - 4] = *(const bf16x8*)(sa + (q - 4) * 2048);
  };
  asm volatile("s_waitcnt vmcnt(12)\n\ts_barrier" ::: "memory");
#pragma unroll
  for (int j = 0; j < 6; ++j) dma(2, j);
#pragma unroll
  for (int q = 0; q < 12; ++q) rdf(0, q);
#pragma unroll
  for (int i = 0; i < 20; ++i) {
    const int t = (i + 1) >> 1;
    if (i & 1) {
      if (t < 9) asm volatile("s_waitcnt vmcnt(12) lgkmcnt(0)\n\ts_barrier" ::: "memory");
      else if (t == 9) asm volatile("s_waitcnt vmcnt(0) lgkmcnt(0)\n\ts_barrier" ::: "memory");
    }
    __builtin_amdgcn_s_waitcnt(0xc07f);
    asm volatile("" : "+v"(Bl[i & 1][0][0]), "+v"(Bl[i & 1][0][1]), "+v"(Bl[i & 1][1][0]), "+v"(Bl[i & 1][1][1]),
                 "+v"(Bl[i & 1][2][0]), "+v"(Bl[i & 1][2][1]), "+v"(Bl[i & 1][3][0]), "+v"(Bl[i & 1][3][1]));
    bf16x8 Bf[4];
#pragma unroll
    for (int n = 0; n < 4; ++n) Bf[n] = __builtin_bit_cast(bf16x8, CAT2(Bl[i & 1][n][0], Bl[i & 1][n][1]));
#pragma unroll
    for (int m = 0; m < 8; ++m) {
      if (i + 1 < 20 && m < 6) { rdf(i + 1, 2 * m); rdf(i + 1, 2 * m + 1); }
      if (t + 2 < 10 && m < 6) dma(t + 2, ((i & 1) ? 0 : 6) + m);
#pragma unroll
      for (int n = 0; n < 4; ++n) acc[m][n] = MFMA16(Bf[n], At[i & 1][m], acc[m][n]);
      __builtin_amdgcn_sched_barrier(0);
    }
  }
  asm volatile("s_nop 7\n\ts_nop 7" ::: "memory");
  __builtin_amdgcn_sched_barrier(0);
  {
    char* eb = smem + 49152 + wid * 16384;
#pragma unroll
    for (int m = 0; m < 8; ++m)
#pragma unroll
      for (int n = 0; n < 4; ++n) {
        u32x2 o;
        o[0] = pack2(acc[m][n][0], acc[m][n][1]);
        o[1] = pack2(acc[m][n][2], acc[m][n][3]);
        *(u32x2*)(eb + m * 2048 + fr * 128 + (((2 * n + (g >> 1)) ^ sw) * 16) + 8 * (g & 1)) = o;
      }
    const int ob = ((rb * 256 + wr * 128 + lr) * NQKV + cb * 128 + wc * 64) * 2;
    const bool skip = rb == 0 && cb >= 32 && wr == 0;
#pragma unroll
    for (int j = 0; j < 16; ++j) {
      if (skip) continue;
      u32x4 o = *(const u32x4*)(eb + j * 1024 + lane * 16);
      __builtin_amdgcn_raw_buffer_store_b128(o, rQ, ob + j * 8 * NQKV * 2 + ((j & 1) ? c1 : c0) * 2, 0, 16);
    }
  }
  DRAIN();
  __syncthreads();
  if (TX == 0) st_agent(flags + FL_TILE + rb * 96 + cb, 1u);
#define TT 0
#define TTA 0
}

__device__ __forceinline__ void task_kv_small(char* smem, char* ws, const u16* __restrict__ Wsel, int cb, int ph) {
  const int TX = opaque_tid();
  const u16* A = (const u16*)(ws + WS_XN);
  u32* flags = (u32*)(ws + WS_FLAG_OFF);
  __amdgpu_buffer_rsrc_t rQ = __builtin_amdgcn_make_buffer_rsrc((void*)(ws + WS_QKV), 0, S_TOK * NQKV * 2, 0x00020000);
  const int wid = TX >> 6, lane = TX & 63, wr = wid >> 1, wc = wid & 1, fr = lane & 15, g = lane >> 4;
  const int lr = lane >> 3, c0 = ((lane & 7) ^ lr) * 8, c1 = ((lane & 7) ^ lr ^ 1) * 8;
  const u16* ga = A + (size_t)(ph * 64 + wid * 16 + lr) * DM;
  const u16* gb = Wsel + (size_t)(16 * wid + (lane >> 4)) * HD + (cb & 31) * 128;
  const int cbA = ((lane & 15) ^ (2 * (lane >> 4))) * 8, cbB = cbA ^ 64;
  auto dma = [&](int t) {
    char* sb = smem + (t % 6) * 24576 + lane * 16;
#pragma unroll
    for (int q = 0; q < 4; ++q)
      __builtin_amdgcn_global_load_lds((const u32*)(gb + (size_t)(t * 64 + 4 * q) * HD + ((q >> 1) ? cbB : cbA)), (u32*)(sb + 8192 + (wid * 4 + q) * 1024), 16, 0, NTA(8));
#pragma unroll
    for (int j = 0; j < 2; ++j)
      __builtin_amdgcn_global_load_lds((const u32*)(ga + j * 8 * DM + t * 64 + (j ? c1 : c0)), (u32*)(sb + (wid * 2 + j) * 1024), 16, 0, 0);
  };
#pragma unroll
  for (int t = 0; t < 5; ++t) dma(t);
  const int sw = (fr & 7) ^ (fr >> 3);
  const int ro0 = fr * 128 + ((g ^ sw) * 16), ro1 = fr * 128 + (((4 + g) ^ sw) * 16);
  u32 bT[3][4];
#pragma unroll
  for (int T = 0; T < 4; ++T) {
    bT[0][T] = (u32)(uintptr_t)((__attribute__((address_space(3))) char*)smem) + 8192 + (8 * g + (fr >> 2)) * 256 +
               (((8 * wc + 2 * T + ((fr >> 1) & 1)) ^ (2 * (fr >> 2) + 8 * (g & 1))) * 16) + 8 * (fr & 1);
    bT[1][T] = bT[0][T] + 49152; bT[2][T] = bT[0][T] + 98304;
  }
  f32x4 acc[2][4];
#pragma unroll
  for (int m = 0; m < 2; ++m)
#pragma unroll
    for (int n = 0; n < 4; ++n) acc[m][n] = ZERO4;
#pragma unroll
  for (int t = 0; t < 10; ++t) {
    if (t <= 5) VMW(24); else if (t == 6) VMW(18); else if (t == 7) VMW(12); else if (t == 8) VMW(6); else VMW(0);
    asm volatile("s_barrier" ::: "memory");
    if (t + 5 < 10) dma(t + 5);
    u32x2 Bl[2][4][2];
    bf16x8 At[2][2];
#pragma unroll
    for (int hf = 0; hf < 2; ++hf)
#pragma unroll
      for (int n = 0; n < 4; ++n) {
        TRA(Bl[hf][n][0], bT[(t % 6) >> 1][n], (t & 1) * 24576 + hf * 8192);
        TRA(Bl[hf][n][1], bT[(t % 6) >> 1][n], (t & 1) * 24576 + hf * 8192 + 1024);
      }
#pragma unroll
    for (int hf = 0; hf < 2; ++hf)
#pragma unroll
      for (int m = 0; m < 2; ++m) At[hf][m] = *(const bf16x8*)(smem + (t % 6) * 24576 + wr * 4096 + m * 2048 + (hf ? ro1 : ro0));
    __builtin_amdgcn_s_waitcnt(0xc07f);
#pragma unroll
    for (int hf = 0; hf < 2; ++hf)
      asm volatile("" : "+v"(Bl[hf][0][0]), "+v"(Bl[hf][0][1]), "+v"(Bl[hf][1][0]), "+v"(Bl[hf][1][1]),
                   "+v"(Bl[hf][2][0]), "+v"(Bl[hf][2][1]), "+v"(Bl[hf][3][0]), "+v"(Bl[hf][3][1]));
#pragma unroll
    for (int hf = 0; hf < 2; ++hf)
#pragma unroll
      for (int m = 0; m < 2; ++m)
#pragma unroll
        for (int n = 0; n < 4; ++n)
          acc[m][n] = MFMA16(__builtin_bit_cast(bf16x8, CAT2(Bl[hf][n][0], Bl[hf][n][1])), At[hf][m], acc[m][n]);
  }
  asm volatile("s_nop 7\n\ts_nop 7" ::: "memory");
  __builtin_amdgcn_sched_barrier(0);
  {
    char* eb = smem + 98304 + wid * 4096;
#pragma unroll
    for (int m = 0; m < 2; ++m)
#pragma unroll
      for (int n = 0; n < 4; ++n) {
        u32x2 o;
        o[0] = pack2(acc[m][n][0], acc[m][n][1]);
        o[1] = pack2(acc[m][n][2], acc[m][n][3]);
        *(u32x2*)(eb + m * 2048 + fr * 128 + (((2 * n + (g >> 1)) ^ sw) * 16) + 8 * (g & 1)) = o;
      }
    const int ob = ((ph * 64 + wr * 32 + lr) * NQKV + cb * 128 + wc * 64) * 2;
#pragma unroll
    for (int j = 0; j < 4; ++j) {
      u32x4 o = *(const u32x4*)(eb + j * 1024 + lane * 16);
      __builtin_amdgcn_raw_buffer_store_b128(o, rQ, ob + j * 8 * NQKV * 2 + ((j & 1) ? c1 : c0) * 2, 0, 16);
    }
  }
  DRAIN();
  __syncthreads();
  if (TX == 0) st_agent(flags + FL_SM + ph * 64 + cb - 32, 1u);
}

__device__ __forceinline__ void task_topk(char* ws) {
  const int TX = opaque_tid();
  const float* r = (const float*)(ws + WS_R);
  u32* idx = (u32*)(ws + WS_IDX);
  u32* flags = (u32*)(ws + WS_FLAG_OFF);
  if (TX < 64) {
    int l = TX;
    float v[32];
#pragma unroll
    for (int i = 0; i < 32; ++i) v[i] = r[l + 64 * i];
    u32 mine = 0;
    for (int k = 0; k < 8; ++k) {
      float bv = v[0]; int bi = l;
#pragma unroll
      for (int i = 1; i < 32; ++i) if (v[i] > bv) { bv = v[i]; bi = l + 64 * i; }
#pragma unroll
      for (int o = 32; o > 0; o >>= 1) {
        float ov = __shfl_xor(bv, o); int oi = __shfl_xor(bi, o);
        if (ov > bv || (ov == bv && oi < bi)) { bv = ov; bi = oi; }
      }
      if (l == k) mine = (u32)bi;
#pragma unroll
      for (int i = 0; i < 32; ++i) if (l + 64 * i == bi) v[i] = -INFINITY;
    }
    if (l < 8) st_agent(idx + l, mine);
    DRAIN();
    if (l == 0) st_agent(flags + FL_TOPK, 1u);
  }
}

__device__ __forceinline__ void task_moe1(char* smem, char* ws, const u16* __restrict__ wg, const u16* __restrict__ wu,
                                          int cbk, bool& dead) {
  const int TX = opaque_tid();
  const u16* xn = (const u16*)(ws + WS_XN);
  u32* idx = (u32*)(ws + WS_IDX);
  u32* hmid = (u32*)(ws + WS_HMID);
  u32* flags = (u32*)(ws + WS_FLAG_OFF);
  float* xs = (float*)smem;
  float* red = xs + 8 * DM;
  float* gu = red + 128 * 260;
  const int t = TX, cg = t & 3, kg = t >> 2;
  u32x4 vg[10], vu[10];
#pragma unroll
  for (int i = 0; i < 10; ++i) {
    vg[i] = *(const u32x4*)(wg + (size_t)(kg + 64 * i) * FF + cbk * 32 + cg * 8);
    vu[i] = *(const u32x4*)(wu + (size_t)(kg + 64 * i) * FF + cbk * 32 + cg * 8);
  }
  if (t == 0) wait_word_ge(flags, FL_TOPK, 1u, dead);
  __syncthreads();
  for (int e = t; e < 8 * DM; e += 256) {
    int m = e / DM, d = e % DM;
    u32 id = ld_agent(idx + m) & (S_TOK - 1);
    xs[d * 8 + m] = bf2f(xn[(size_t)id * DM + d]);
  }
  __syncthreads();
  float ag[8][8], au[8][8];
#pragma unroll
  for (int m = 0; m < 8; ++m)
#pragma unroll
    for (int j = 0; j < 8; ++j) { ag[m][j] = 0.f; au[m][j] = 0.f; }
#pragma unroll
  for (int i = 0; i < 10; ++i) {
    f32x4 x0 = *(const f32x4*)(xs + (kg + 64 * i) * 8), x1 = *(const f32x4*)(xs + (kg + 64 * i) * 8 + 4);
#pragma unroll
    for (int j = 0; j < 8; ++j) {
      float gw = __uint_as_float((j & 1) ? (vg[i][j >> 1] & 0xffff0000u) : (vg[i][j >> 1] << 16));
      float uw = __uint_as_float((j & 1) ? (vu[i][j >> 1] & 0xffff0000u) : (vu[i][j >> 1] << 16));
#pragma unroll
      for (int m = 0; m < 8; ++m) {
        float xv = m < 4 ? x0[m & 3] : x1[m & 3];
        ag[m][j] += xv * gw; au[m][j] += xv * uw;
      }
    }
  }
#pragma unroll
  for (int m = 0; m < 8; ++m)
#pragma unroll
    for (int j = 0; j < 8; ++j) { red[(m * 8 + j) * 260 + t] = ag[m][j]; red[(64 + m * 8 + j) * 260 + t] = au[m][j]; }
  __syncthreads();
  {
    const int q = t >> 1, hf = t & 1;
    f32x4 sacc = ZERO4;
#pragma unroll 8
    for (int k = 0; k < 32; ++k) sacc += *(const f32x4*)(red + q * 260 + hf * 128 + k * 4);
#pragma unroll
    for (int c = 0; c < 4; ++c) sacc[c] += __shfl_xor(sacc[c], 1);
    if (hf == 0) {
#pragma unroll
      for (int c = 0; c < 4; ++c) gu[(q >> 3) * 32 + c * 8 + (q & 7)] = sacc[c];
    }
  }
  __syncthreads();
  {
    const int m = t & 7, n = t >> 3;
    float g = rbf(gu[m * 32 + n]), u = rbf(gu[(8 + m) * 32 + n]);
    float sg = rbf(g / (1.0f + __expf(-g)));
    st_agent(hmid + (cbk * 32 + n) * 8 + m, __float_as_uint(rbf(sg * u)));
  }
  DRAIN();
  __syncthreads();
  if (t == 0) add_agent(flags + FL_MOE1, 1u);
}

__device__ __forceinline__ void task_moe2(char* smem, char* ws, const u16* __restrict__ wd, int cb, bool& dead) {
  const int TX = opaque_tid();
  u32* idx = (u32*)(ws + WS_IDX);
  u32* upd = (u32*)(ws + WS_UPD);
  const float* r = (const float*)(ws + WS_R);
  u32* flags = (u32*)(ws + WS_FLAG_OFF);
  __amdgpu_buffer_rsrc_t rH = __builtin_amdgcn_make_buffer_rsrc((void*)(ws + WS_HMID), 0, 8 * FF * 4, 0x00020000);
  float* hs = (float*)smem;
  float* red = hs + 8 * FF;
  float* yo = red + 64 * 260;
  const int t = TX, cg = t & 3, kg = t >> 2;
  u32x4 vd[27];
#pragma unroll
  for (int i = 0; i < 27; ++i) vd[i] = *(const u32x4*)(wd + (size_t)(kg + 64 * i) * DM + cb * 32 + cg * 8);
  if (t == 0) wait_word_ge(flags, FL_MOE1, 54u, dead);
  __syncthreads();
  for (int e = t; e < 2 * FF; e += 256) *(u32x4*)(hs + e * 4) = __builtin_amdgcn_raw_buffer_load_b128(rH, e * 16, 0, 16);
  __syncthreads();
  float a[8][8];
#pragma unroll
  for (int m = 0; m < 8; ++m)
#pragma unroll
    for (int j = 0; j < 8; ++j) a[m][j] = 0.f;
#pragma unroll
  for (int i = 0; i < 27; ++i) {
    f32x4 x0 = *(const f32x4*)(hs + (kg + 64 * i) * 8), x1 = *(const f32x4*)(hs + (kg + 64 * i) * 8 + 4);
#pragma unroll
    for (int j = 0; j < 8; ++j) {
      float dw = __uint_as_float((j & 1) ? (vd[i][j >> 1] & 0xffff0000u) : (vd[i][j >> 1] << 16));
#pragma unroll
      for (int m = 0; m < 8; ++m) a[m][j] += (m < 4 ? x0[m & 3] : x1[m & 3]) * dw;
    }
  }
#pragma unroll
  for (int m = 0; m < 8; ++m)
#pragma unroll
    for (int j = 0; j < 8; ++j) red[(m * 8 + j) * 260 + t] = a[m][j];
  __syncthreads();
  {
    const int q = t >> 2, qf = t & 3;
    f32x4 sacc = ZERO4;
#pragma unroll 8
    for (int k = 0; k < 16; ++k) sacc += *(const f32x4*)(red + q * 260 + qf * 64 + k * 4);
#pragma unroll
    for (int c = 0; c < 4; ++c) { sacc[c] += __shfl_xor(sacc[c], 1); sacc[c] += __shfl_xor(sacc[c], 2); }
    if (qf == 0) {
#pragma unroll
      for (int c = 0; c < 4; ++c) yo[(q >> 3) * 32 + c * 8 + (q & 7)] = sacc[c];
    }
  }
  __syncthreads();
  {
    const int m = t >> 5, n = t & 31;
    float y = rbf(yo[m * 32 + n]);
    float rv = r[ld_agent(idx + m) & (S_TOK - 1)];
    float sg = rbf(1.0f / (1.0f + __expf(-rv)));
    st_agent(upd + m * DM + cb * 32 + n, __float_as_uint(rbf(y * sg)));
  }
  DRAIN();
  __syncthreads();
  if (t == 0) add_agent(flags + FL_MOE2, 1u);
}

__device__ __forceinline__ void task_attn(char* smem, char* ws, int s, int hg, bool& dead, const u32* stp) {
  const int TX = opaque_tid();
  const u16* qkvx = (const u16*)(ws + WS_QKV);
  u32* flags = (u32*)(ws + WS_FLAG_OFF);
  __amdgpu_buffer_rsrc_t rO = __builtin_amdgcn_make_buffer_rsrc((void*)(ws + WS_OX), 0, S_TOK * HD * 2, 0x00020000);
  int w = TX >> 6, l = TX & 63, fr = l & 15, g = l >> 4;
  if (w == 0) {
    const int rb = s >> 3;
    int word = l < 12 ? FL_TILE + rb * 96 + (l >> 2) * 32 + hg * 4 + (l & 3) : FL_SM + ((l - 12) >> 2) * 32 + hg * 4 + (l & 3);
    u32 spins = 0;
    for (;;) {
      u32 v = (l < 12 || (l < 28 && rb == 0)) ? ld_agent(flags + word) : 1u;
      if (__all(v >= 1u)) break;
      __builtin_amdgcn_s_sleep(WRK_GAP2);
      if (dead || ++spins > 16 * SPIN_CAP) { if (l == 0) atomicOr(flags + FL_ERR, 32u); dead = true; break; }
    }
  }
  __syncthreads();
  u16(*Vs)[136] = (u16(*)[136])(smem + w * 32 * 136 * 2);
  const int h = hg * 4 + w;
  const u16* Qb = qkvx + (size_t)(s * SEG) * NQKV + h * DH;
  __amdgpu_buffer_rsrc_t rQ = __builtin_amdgcn_make_buffer_rsrc((void*)(ws + WS_QKV), 0, S_TOK * NQKV * 2, 0x00020000);
  const int qo = (s * SEG * NQKV + h * DH) * 2;
  const u16* Kb = Qb + HD;
  const u16* Vb = Qb + 2 * HD;
  const u32 va = (u32)(uintptr_t)((__attribute__((address_space(3))) char*)smem) + w * 32 * 272 + (fr >> 2) * 272 + 16 * (fr & 3);
  __amdgpu_buffer_rsrc_t rK = __builtin_amdgcn_make_buffer_rsrc((void*)(ws + WS_KVM), 0, NSEG * MEMR * 8192 * 2, 0x00020000);
  bf16x8 b[4][2];
  float pr[2][12];
  bf16x8 vx[8];
  {
    u32x4 vv[8];
    bf16x8 a[4][2];
#pragma unroll
    for (int i = 0; i < 8; ++i) vv[i] = __builtin_amdgcn_raw_buffer_load_b128(rQ, qo + ((4 * i + g) * NQKV + 2 * HD + fr * 8) * 2, 0, 16);
#pragma unroll
    for (int ks = 0; ks < 4; ++ks)
#pragma unroll
      for (int t = 0; t < 2; ++t) {
        a[ks][t] = __builtin_bit_cast(bf16x8, __builtin_amdgcn_raw_buffer_load_b128(rQ, qo + ((16 * t + fr) * NQKV + HD + 32 * ks + 8 * g) * 2, 0, 16));
        b[ks][t] = __builtin_bit_cast(bf16x8, __builtin_amdgcn_raw_buffer_load_b128(rQ, qo + ((16 * t + fr) * NQKV + 32 * ks + 8 * g) * 2, 0, 16));
      }
#pragma unroll
    for (int i = 0; i < 8; ++i) *(u32x4*)(&Vs[4 * i + g][fr * 8]) = vv[i];
    f32x4 sc[2][2] = {};
#pragma unroll
    for (int ks = 0; ks < 4; ++ks)
#pragma unroll
      for (int it = 0; it < 2; ++it)
#pragma unroll
        for (int t = 0; t < 2; ++t) sc[it][t] = MFMA16(a[ks][t], b[ks][it], sc[it][t]);
#pragma unroll
    for (int it = 0; it < 2; ++it)
#pragma unroll
      for (int t = 0; t < 2; ++t)
#pragma unroll
        for (int r = 0; r < 4; ++r) {
          float v = rbf(rbf(sc[it][t][r]) * 0.08838834764831845f);
          pr[it][4 * t + r] = (16 * t + 4 * g + r > 16 * it + fr) ? -INFINITY : v;
        }
#pragma unroll
    for (int T = 0; T < 8; ++T) vx[T] = trd2(va + 4 * g * 272 + 64 * (T >> 1) + 8 * (T & 1), va + (16 + 4 * g) * 272 + 64 * (T >> 1) + 8 * (T & 1));
  }
  float mxx[2], sxp[2];
#pragma unroll
  for (int it = 0; it < 2; ++it) {
    float mx = pr[it][0];
#pragma unroll
    for (int q = 1; q < 8; ++q) mx = fmaxf(mx, pr[it][q]);
    mx = red_max_rows(mx);
    float sum = 0.f;
#pragma unroll
    for (int q = 0; q < 8; ++q) { float e = __expf(pr[it][q] - mx); pr[it][q] = e; sum += e; }
    mxx[it] = mx; sxp[it] = sum;
  }
  asm volatile("" : "+v"(mxx[0]), "+v"(mxx[1]), "+v"(sxp[0]), "+v"(sxp[1]));
#pragma unroll
  for (int it = 0; it < 2; ++it)
    asm volatile("" : "+v"(pr[it][0]), "+v"(pr[it][1]), "+v"(pr[it][2]), "+v"(pr[it][3]), "+v"(pr[it][4]), "+v"(pr[it][5]), "+v"(pr[it][6]), "+v"(pr[it][7]));
  asm volatile("" : "+v"(vx[0]), "+v"(vx[1]), "+v"(vx[2]), "+v"(vx[3]), "+v"(vx[4]), "+v"(vx[5]), "+v"(vx[6]), "+v"(vx[7]));
  const bool direct = s >= NSEG - NDIR;
  {
    if (w == 0) {
      u32 spins = 0;
      while (!__all(ld_agent(flags + FLS(direct ? s - GATE : s, hg, l & 7)) != 0u)) {
        if (!direct) __builtin_amdgcn_s_sleep(WRK_GAP); else __builtin_amdgcn_s_sleep(WRK_GAPD);
        if (dead || ++spins > 64 * SPIN_CAP) { if (l == 0) atomicOr(flags + FL_ERR, 32u); dead = true; break; }
      }
      if (direct) {
        u32 est;
        if (spins) __builtin_amdgcn_s_sleep(GSL);
        pace_tail<FS_J>(stp, flags + FLS(s, hg, 0), 0, s, s + 1 < NSEG ? DT_KV : DT_KVL, est);
      }
    }
    __syncthreads();
  }
  bf16x4 vm[8];
  {
    bf16x8 km[4];
    {
      u32x4 kq[4], vq[2];
      const int ko = ((s * MEMR + (fr & 7)) * 8192 + h * DH + 8 * g) * 2, vo = ((s * MEMR + g) * 8192 + HD + h * DH + 8 * fr) * 2;
      u32 spins = 0;
      for (;;) {
#pragma unroll
        for (int ks = 0; ks < 4; ++ks) kq[ks] = __builtin_amdgcn_raw_buffer_load_b128(rK, ko + 64 * ks, 0, 16);
#pragma unroll
        for (int j = 0; j < 2; ++j) vq[j] = __builtin_amdgcn_raw_buffer_load_b128(rK, vo + j * 4 * 8192 * 2, 0, 16);
        if (!direct) break;
        u32 mxu = 0;
#pragma unroll
        for (int q = 0; q < 4; ++q) mxu = max(mxu, max(max(max(kq[0][q], kq[1][q]), max(kq[2][q], kq[3][q])), max(vq[0][q], vq[1][q])));
        if (__all(mxu != ~0u)) break;
        asm volatile("" ::: "memory");
        __builtin_amdgcn_s_sleep(4);
        if (dead || ++spins > 64 * SPIN_CAP) { if (l == 0) atomicOr(flags + FL_ERR, 32u); dead = true; break; }
      }
#pragma unroll
      for (int ks = 0; ks < 4; ++ks) km[ks] = __builtin_bit_cast(bf16x8, kq[ks]);
#pragma unroll
      for (int j = 0; j < 2; ++j) *(u32x4*)(&Vs[4 * j + g][fr * 8]) = vq[j];
#pragma unroll
      for (int T = 0; T < 8; ++T) vm[T] = __builtin_bit_cast(bf16x4, trd(va + 4 * (g & 1) * 272 + 64 * (T >> 1) + 8 * (T & 1)));
    }
    f32x4 sm[2] = {};
#pragma unroll
    for (int ks = 0; ks < 4; ++ks)
#pragma unroll
      for (int it = 0; it < 2; ++it) sm[it] = MFMA16(km[ks], b[ks][it], sm[it]);
#pragma unroll
    for (int it = 0; it < 2; ++it)
#pragma unroll
      for (int r = 0; r < 4; ++r) pr[it][8 + r] = g < 2 ? rbf(rbf(sm[it][r]) * 0.08838834764831845f) : -INFINITY;
  }
  u32x4 pbx[2];
  u32x2 pbm[2];
#pragma unroll
  for (int it = 0; it < 2; ++it) {
    float mx = fmaxf(fmaxf(pr[it][8], pr[it][9]), fmaxf(pr[it][10], pr[it][11]));
    mx = fmaxf(red_max_rows(mx), mxx[it]);
    const float fx = __expf(mxx[it] - mx);
    float sum = sxp[it] * fx;
#pragma unroll
    for (int q = 8; q < 12; ++q) { float e = __expf(pr[it][q] - mx); pr[it][q] = e; sum += e; }
    sum = red_sum_rows(sum);
    const float inv = 1.0f / sum, fi = fx * inv;
#pragma unroll
    for (int q = 0; q < 4; ++q) pbx[it][q] = pack2(pr[it][2 * q] * fi, pr[it][2 * q + 1] * fi);
#pragma unroll
    for (int q = 0; q < 2; ++q) pbm[it][q] = pack2(pr[it][8 + 2 * q] * inv, pr[it][9 + 2 * q] * inv);
  }
  {
    f32x4 oa[8][2];
    asm volatile("s_nop 1" : "+v"(pbx[0]), "+v"(pbx[1]), "+v"(pbm[0]), "+v"(pbm[1]));
#pragma unroll
    for (int T = 0; T < 8; ++T)
#pragma unroll
      for (int it = 0; it < 2; ++it) { MFMA_V0(oa[T][it], vx[T], pbx[it]); __builtin_amdgcn_sched_barrier(0); }
#pragma unroll
    for (int T = 0; T < 8; ++T)
#pragma unroll
      for (int it = 0; it < 2; ++it) { MFMA_K16(oa[T][it], vm[T], pbm[it]); __builtin_amdgcn_sched_barrier(0); }
    asm volatile("s_nop 7" : "+v"(oa[0][0]), "+v"(oa[1][0]));
#pragma unroll
    for (int p = 0; p < 4; ++p)
#pragma unroll
      for (int it = 0; it < 2; ++it) {
        if (p == 3) asm volatile("s_nop 3" : "+v"(oa[6][it]), "+v"(oa[7][it]));
        u32x4 o;
        o[0] = pack2(oa[2 * p][it][0], oa[2 * p][it][1]); o[1] = pack2(oa[2 * p][it][2], oa[2 * p][it][3]);
        o[2] = pack2(oa[2 * p + 1][it][0], oa[2 * p + 1][it][1]); o[3] = pack2(oa[2 * p + 1][it][2], oa[2 * p + 1][it][3]);
#pragma unroll
        for (int j = 0; j < 4; ++j) o[j] = min(o[j], 0xFFFFFFFEu);
        *(u32x4*)(&Vs[16 * it + fr][32 * p + 8 * g]) = o;
        __builtin_amdgcn_sched_barrier(0);
      }
#pragma unroll
    for (int i = 0; i < 8; ++i) {
      const u32x4 o = *(const u32x4*)(&Vs[4 * i + g][fr * 8]);
      __builtin_amdgcn_raw_buffer_store_b128(o, rO, ((s * SEG + 4 * i + g) * HD + h * DH + fr * 8) * 2, 0, 16);
    }
  }
  DRAIN();
  __syncthreads();
  if (TX == 0) st_agent(flags + FL_ATTN + s * 16 + hg, 1u);
}

__device__ __forceinline__ void role_oproj(char* smem, char* ws, const u16* __restrict__ x, u16* __restrict__ out, const u16* __restrict__ wo,
                                           int nb, int ph, bool& dead, const u32* stp) {
  const int TX = opaque_tid();
  const u16* ox = (const u16*)(ws + WS_OX);
  const u16* xn = (const u16*)(ws + WS_XN);
  const int* idx = (const int*)(ws + WS_IDX);
  const float* upd = (const float*)(ws + WS_UPD);
  u32* flags = (u32*)(ws + WS_FLAG_OFF);
  const int w = TX >> 6, l = TX & 63, fr = l & 15, g = l >> 4;
  const int lr = l >> 3, c0 = ((l & 7) ^ lr) * 8, c1 = ((l & 7) ^ lr ^ 1) * 8;
  const int sw = (fr & 7) ^ (fr >> 3);
  const int ro0 = fr * 128 + ((g ^ sw) * 16), ro1 = fr * 128 + (((4 + g) ^ sw) * 16);
  char* ring = smem + w * 36864;
  auto stage = [&](const u16* gp, int t) {
    char* sb = ring + (t & 7) * 4096 + l * 16;
#pragma unroll
    for (int j = 0; j < 4; ++j)
      __builtin_amdgcn_global_load_lds((const u32*)(gp + (size_t)j * 8 * HD + t * 64 + ((j & 1) ? c1 : c0)), (u32*)(sb + j * 1024), 16, 0, OPA2);
  };
  bf16x8 bq[2][32];
  {
    const u16* gb = wo + (size_t)(w * 1024 + (l >> 2)) * DM + nb * 32 + ((l & 3) ^ (2 * (l >> 5))) * 8;
    const u32 fo = (u32)(uintptr_t)((__attribute__((address_space(3))) char*)ring) + (8 * g + (fr >> 2)) * 64 +
                   ((((fr >> 1) & 1) ^ (2 * (g & 1))) * 16) + 8 * (fr & 1);
#pragma unroll
    for (int bt = 0; bt < 2; ++bt) {
#pragma unroll
      for (int t = 0; t < 8; ++t)
#pragma unroll
        for (int i = 0; i < 4; ++i)
          __builtin_amdgcn_global_load_lds((const u32*)(gb + (size_t)((bt * 8 + t) * 64 + 16 * i) * DM), (u32*)(ring + t * 4096 + i * 1024 + l * 16), 16, 0, 0);
      asm volatile("s_waitcnt vmcnt(0)" ::: "memory");
#pragma unroll
      for (int t = 0; t < 8; ++t)
#pragma unroll
        for (int n = 0; n < 2; ++n) {
          const u32 a = (fo ^ (32 * n)) + t * 4096;
          bq[n][(bt * 8 + t) * 2] = trd2(a, a + 256);
          bq[n][(bt * 8 + t) * 2 + 1] = trd2(a + 2048, a + 2304);
        }
      asm volatile("s_waitcnt lgkmcnt(0)" ::: "memory");
    }
  }
  if (w == 0) {
    u32 spins = 0;
    while (ld_agent(flags + FL_MOE2) < 20u) {
      __builtin_amdgcn_s_sleep(WRK_GAP2);
      if (dead || ++spins > 16 * SPIN_CAP) { if (l == 0) atomicOr(flags + FL_ERR, 64u); dead = true; break; }
    }
    __builtin_amdgcn_fence(__ATOMIC_ACQUIRE, "agent");
    DRAIN();
  }
  __syncthreads();
  int idr[8];
#pragma unroll
  for (int k = 0; k < 8; ++k) idr[k] = idx[k];
  __builtin_amdgcn_s_waitcnt(0);
  u64 rdy = 0;
  u32 est = 0;
#pragma unroll 1
  for (int si = ph; si < NSEG - NSPL + 4 * NSPL; si += 4) {
    const bool split = si >= NSEG - NSPL;
    const int s = split ? NSEG - NSPL + ((si - (NSEG - NSPL)) >> 2) : si;
    const int row = split ? s * SEG + 8 * ph + (fr & 7) : s * SEG + 16 * (w >> 1) + fr, col = nb * 32 + 16 * (w & 1) + 4 * g;
    int sel = -1;
#pragma unroll
    for (int k = 0; k < 8; ++k) if (idr[k] == row) sel = k;
    if (w == 0) {
      u32 spins = 0;
      if (s >= NSEG - NSEN) {
        if ((MV & 4) && s == NSEG - NSEN)
          while (!__all(ld_agent(flags + FL_ATTN + (NSEG - NSEN - 4 + ((l >> 3) & 3)) * 16 + (l & 7)) != 0u)) {
            __builtin_amdgcn_s_sleep(8);
            if (dead || ++spins > 16 * SPIN_CAP) { if (l == 0) atomicOr(flags + FL_ERR, 64u); dead = true; break; }
          }
        rdy >>= 8;
        if ((rdy & 0xFFu) != 0xFFu) {
          if (s > NSEG - NSEN) wait_until(est);
          est = rt_now();
          rdy = pace_tail<1>(stp, flags + FL_ATTN + s * 16, 16, s, s + 2 < NSEG ? DT_OX : s + 1 < NSEG ? DT_OXM : DT_OXL, est);
        }
      } else
      while (!__all(ld_agent(flags + FL_ATTN + s * 16 + (l & 7)) != 0u)) {
        if (!split) __builtin_amdgcn_s_sleep(WRK_GAP2); else __builtin_amdgcn_s_sleep(SPG);
        if (dead || ++spins > 16 * SPIN_CAP) { if (l == 0) atomicOr(flags + FL_ERR, 64u); dead = true; break; }
      }
#define OPAUX 16
    }
    __syncthreads();
    const u32x2 xv = *(const u32x2*)(x + (size_t)row * DM + col);
    const u32x2 nv = *(const u32x2*)(xn + (size_t)row * DM + col);
    const f32x4 uv = *(const f32x4*)(upd + (sel < 0 ? 0 : sel) * DM + col);
    __builtin_amdgcn_sched_barrier(0);
    f32x4 acc[2][2] = {};
    if (split) {
      const u16* ga = ox + (size_t)(s * SEG + 8 * ph + lr) * HD + w * 1024 + c0;
      const int r0 = (fr & 7) * 128 + ((g ^ (fr & 7)) * 16), r1 = (fr & 7) * 128 + (((4 + g) ^ (fr & 7)) * 16);
      u32 spins = 0;
      for (;;) {
#pragma unroll
        for (int t = 0; t < 16; ++t) __builtin_amdgcn_global_load_lds((const u32*)(ga + t * 64), (u32*)(ring + t * 1024 + l * 16), 16, 0, 16);
        u32 mxu = 0;
#pragma unroll
        for (int t = 0; t < 16; ++t) {
          if (t == 0) VMW(12); else if (t == 4) VMW(8); else if (t == 8) VMW(4); else if (t == 12) VMW(0);
          bf16x8 a0 = *(const bf16x8*)(ring + t * 1024 + r0), a1 = *(const bf16x8*)(ring + t * 1024 + r1);
          const u32x4 u0 = __builtin_bit_cast(u32x4, a0), u1 = __builtin_bit_cast(u32x4, a1);
          mxu = max(max(mxu, max(max(u0[0], u0[1]), max(u0[2], u0[3]))), max(max(u1[0], u1[1]), max(u1[2], u1[3])));
#pragma unroll
          for (int n = 0; n < 2; ++n) {
            acc[0][n] = MFMA16(bq[n][2 * t], a0, acc[0][n]);
            acc[1][n] = MFMA16(bq[n][2 * t + 1], a1, acc[1][n]);
          }
        }
        if (__builtin_expect(__all(mxu != ~0u), 1)) break;
        asm volatile("s_waitcnt lgkmcnt(0)" ::: "memory");
        __builtin_amdgcn_s_sleep(8);
        if (dead || ++spins > 16 * SPIN_CAP) { if (l == 0) atomicOr(flags + FL_ERR, 64u); dead = true; break; }
#pragma unroll
        for (int n = 0; n < 2; ++n) { acc[0][n] = ZERO4; acc[1][n] = ZERO4; }
      }
#pragma unroll
      for (int n = 0; n < 2; ++n) acc[0][n] += acc[1][n];
    } else {
      const u16* ga = ox + (size_t)(s * SEG + lr) * HD + w * 1024;
#pragma unroll
      for (int t = 0; t < 8; ++t) stage(ga, t);
#pragma unroll
      for (int t = 0; t < 16; ++t) {
        if (t <= 8) asm volatile("s_waitcnt vmcnt(28)" ::: "memory");
        else if (t == 9) asm volatile("s_waitcnt vmcnt(24)" ::: "memory");
        else if (t == 10) asm volatile("s_waitcnt vmcnt(20)" ::: "memory");
        else if (t == 11) asm volatile("s_waitcnt vmcnt(16)" ::: "memory");
        else if (t == 12) asm volatile("s_waitcnt vmcnt(12)" ::: "memory");
        else if (t == 13) asm volatile("s_waitcnt vmcnt(8)" ::: "memory");
        else if (t == 14) asm volatile("s_waitcnt vmcnt(4)" ::: "memory");
        else asm volatile("s_waitcnt vmcnt(0)" ::: "memory");
        const char* sa = ring + (t & 7) * 4096;
        bf16x8 a00 = *(const bf16x8*)(sa + ro0), a01 = *(const bf16x8*)(sa + 2048 + ro0);
        bf16x8 a10 = *(const bf16x8*)(sa + ro1), a11 = *(const bf16x8*)(sa + 2048 + ro1);
        asm volatile("s_waitcnt lgkmcnt(0)" : "+v"(a00), "+v"(a01), "+v"(a10), "+v"(a11) :: "memory");
        if (t + 8 < 16) stage(ga, t + 8);
#pragma unroll
        for (int n = 0; n < 2; ++n) {
          acc[0][n] = MFMA16(bq[n][2 * t], a00, acc[0][n]);
          acc[1][n] = MFMA16(bq[n][2 * t], a01, acc[1][n]);
          acc[0][n] = MFMA16(bq[n][2 * t + 1], a10, acc[0][n]);
          acc[1][n] = MFMA16(bq[n][2 * t + 1], a11, acc[1][n]);
        }
      }
    }
    __syncthreads();
    f32x4* red = (f32x4*)smem;
#pragma unroll
    for (int it = 0; it < 2; ++it)
#pragma unroll
      for (int n = 0; n < 2; ++n) red[((w * 2 + it) * 2 + n) * 64 + l] = acc[it][n];
    __syncthreads();
    {
      f32x4 v = (red[(0 * 4 + w) * 64 + l] + red[(1 * 4 + w) * 64 + l]) + (red[(2 * 4 + w) * 64 + l] + red[(3 * 4 + w) * 64 + l]);
      u16 res[4];
#pragma unroll
      for (int j = 0; j < 4; ++j) {
        const float xx = bf2f((u16)((xv[j >> 1] >> ((j & 1) * 16)) & 0xffff));
        const float nn = bf2f((u16)((nv[j >> 1] >> ((j & 1) * 16)) & 0xffff));
        const float bo = sel >= 0 ? rbf(nn + uv[j]) : nn;
        res[j] = f2bf(rbf(xx + rbf(v[j])) + bo);
      }
      u32x2 o;
      o[0] = (u32)res[0] | ((u32)res[1] << 16);
      o[1] = (u32)res[2] | ((u32)res[3] << 16);
      if (!split || (w < 2 && fr < 8)) *(u32x2*)(out + (size_t)row * DM + col) = o;
    }
    __syncthreads();
  }
}

#define T_A 64
#define T_TOPK T_A
#define T_B (T_TOPK + 1)
#define T_MOE1 (T_B + 128)
#define T_C (T_MOE1 + 54)
#define T_MOE2 (T_C + 128)
#define T_D (T_MOE2 + 20)
#define T_SEG (T_D + 448)

__global__ __launch_bounds__(256, 1) void k_main(const u16* __restrict__ x, const u16* __restrict__ mem0,
                                                 const u16* __restrict__ wg, const u16* __restrict__ wu,
                                                 const u16* __restrict__ wd, u16* __restrict__ out, char* ws,
                                                 const u16* __restrict__ wq, const u16* __restrict__ wk_,
                                                 const u16* __restrict__ wv, const u16* __restrict__ wo) {
  extern __shared__ __attribute__((aligned(16))) char smem[];
  const int b = blockIdx.x;
  if (b < N_REC) {
    role_recur(smem, ws, mem0, wq, wk_, wv, wo, b);
  } else if (b < N_REC + N_RED) {
    {
      const int j = b - N_REC < 64 ? b - N_REC : b - N_REC - 16;
      task_kv_small(smem, ws, j < 32 ? wk_ : wv, 32 + j, b - N_REC < 64 ? 0 : 1);
    }
    role_reduce(smem, ws, b - N_REC);
  } else {
    bool dead = false;
    const int wk = b - (N_REC + N_RED);
    const u32* stp;
    {
      const u32 Xw = cal_x(((const u32*)(ws + WS_CAL))[threadIdx.x & 63]);
      const u32 xcc = __builtin_amdgcn_s_getreg(20 | (31 << 11)) & 7u;
      stp = (const u32*)(CW_PTR(ws) + CW_TS + (((((xcc >> 2) & 1u) | ((xcc & 1u) << 1)) ^ Xw) << 13));
    }
    if (wk >= 64) {
      task_kv_small(smem, ws, wk < 96 ? wk_ : wv, wk - 32, 1);
      __syncthreads();
    }
#define KT 0
#define KTA 16
    auto ktouch = [&](int it) {
      if (KT && threadIdx.x < 64)
        __builtin_amdgcn_global_load_lds((const u32*)(ws + WS_KVM + (size_t)((it * N_WRK + wk) * 64 + threadIdx.x) * 128),
                                         (u32*)(smem + 158720 + threadIdx.x * 4), 4, 0, KTA);
    };
    int it = 0;
    for (int id = wk; id < T_SEG; id += N_WRK, ++it) {
      int rb = -1, cb = 0;
      if (it < 8) ktouch(it);
      if (id < T_A) { rb = 0; cb = 32 + id; }
      else if (id < T_B) task_topk(ws);
      else if (id < T_MOE1) { int q = id - T_B; rb = 1 + (q >> 6); cb = 32 + (q & 63); }
      else if (id < T_C) task_moe1(smem, ws, wg, wu, id - T_MOE1, dead);
      else if (id < T_MOE2) { int q = id - T_C; if (q < 64) { rb = q >> 5; cb = q & 31; } else { rb = 3; cb = 32 + (q - 64); } }
      else if (id < T_D) task_moe2(smem, ws, wd, id - T_MOE2, dead);
      else { int q = id - T_D; if (q < 256) { rb = 4 + (q >> 6); cb = 32 + (q & 63); } else { q -= 256; rb = 2 + (q >> 5); cb = q & 31; } }
      cb = (cb & ~7) | (id & 7);
      if (rb >= 0) task_qkv_tile(smem, ws, cb < 32 ? wq : (cb < 64 ? wk_ : wv), rb, cb);
      __syncthreads();
    }
    for (; it < 8; ++it) ktouch(it);
    if (wk < 32) {
      if (MV & 4) {
        __amdgpu_buffer_rsrc_t rO = __builtin_amdgcn_make_buffer_rsrc((void*)(ws + WS_OX), 0, S_TOK * HD * 2, 0x00020000);
        const u32x4 z = {~0u, ~0u, ~0u, ~0u};
        const int t = threadIdx.x;
        for (int s = NSEG - NSEN + (wk >> 3); s < NSEG; s += 4)
#pragma unroll
          for (int i = 0; i < 8; ++i)
            __builtin_amdgcn_raw_buffer_store_b128(z, rO, ((s * SEG + 4 * i + (t >> 6)) * HD + (wk & 7) * 512 + (t & 63) * 8) * 2, 0, 16);
      }
      for (int s = wk >> 3; s < NSEG; s += 4) {
        task_attn(smem, ws, s, wk & 7, dead, stp);
        __syncthreads();
      }
    } else {
      role_oproj(smem, ws, x, out, wo, (wk - 32) % 20, (wk - 32) / 20, dead, stp);
    }
  }
}

extern "C" void kernel_launch(void* const* d_in, const int* in_sizes, int n_in, void* d_out, int out_size,
                              void* d_ws, size_t ws_size, hipStream_t stream) {
  const u16* x = (const u16*)d_in[0];
  const u16* mem0 = (const u16*)d_in[1];
  const u16* wq = (const u16*)d_in[2];
  const u16* wk = (const u16*)d_in[3];
  const u16* wv = (const u16*)d_in[4];
  const u16* wo = (const u16*)d_in[5];
  const u16* wr = (const u16*)d_in[8];
  const u16* wg = (const u16*)d_in[9];
  const u16* wu = (const u16*)d_in[10];
  const u16* wd = (const u16*)d_in[11];
  u16* out = (u16*)d_out;
  char* ws = (char*)d_ws;
    k_prep<<<PB_E, 256, 0, stream>>>(x, wr, ws);
  (void)hipFuncSetAttribute((const void*)k_main, hipFuncAttributeMaxDynamicSharedMemorySize, LDS_TOTAL);
  k_main<<<N_REC + N_RED + N_WRK, 256, LDS_TOTAL, stream>>>(x, mem0, wg, wu, wd, out, ws, wq, wk, wv, wo);
}
```
